```python
import torch

def _softmax(x, axis=-1):
    return torch.softmax(x, dim=axis)
def _gelu(x):
    return torch.nn.functional.gelu(x, approximate="tanh")
def _silu(x):
    return torch.nn.functional.silu(x)
def _sigmoid(x):
    return torch.sigmoid(x)
def _layernorm(x, axis=-1, eps=1e-5):
    m = x.mean(dim=axis, keepdim=True)
    v = x.var(dim=axis, keepdim=True, unbiased=False)
    return (x - m) / torch.sqrt(v + eps)
def _scatter_add(tgt, idx, src, axis=0):
    return tgt.clone().index_add_(axis, idx, src)
def _segment_sum(src, seg, n):
    out = torch.zeros((n,) + src.shape[1:], dtype=src.dtype)
    return out.index_add_(0, seg, src)
def _pad(x, pw):
    flat = tuple(v for pair in reversed(pw) for v in pair)
    return torch.nn.functional.pad(x, flat)

kernel_name = 'gated_mix__sliding_block_cached__dense_activation_relu__decode'

def setup_inputs():

    _w = (lambda a: (lambda t: t.to(torch.bfloat16) if t.is_floating_point() else t)(torch.as_tensor(a)))
    def _A_setup():
        B, Sq, D = 4, 1, 640
        Hq, Hkv, Sk, Dh = 32, 32, 4096, 192
        assert Hq % Hkv == 0, (Hq, Hkv)
        return {
            "x": _w((lambda *s: torch.randn(*s, dtype=torch.bfloat16) * 0.05)(B, Sq, D)),
            "k_cache": _w((lambda *s: torch.randn(*s, dtype=torch.bfloat16) * 3.2)(B, Hkv, Sk, Dh)),
            "v_cache": _w((lambda *s: torch.randn(*s, dtype=torch.bfloat16) * 1.6)(B, Hkv, Sk, Dh)),
            "wq": _w((lambda *s: torch.randn(*s, dtype=torch.bfloat16) * 3.2)(D, Hq * Dh) * (D ** -0.5)),
            "wo": _w((lambda *s: torch.randn(*s, dtype=torch.bfloat16) * 1.6)(Hq * Dh, D) * ((Hq * Dh) ** -0.5)),
        }
    _A_in = _A_setup()

    def _B_setup():
        B, S, D, Dff = 4, 1, 640, 1536
        return {
            "x": _w((lambda *s: torch.randn(*s, dtype=torch.bfloat16) * 0.05)(B, S, D)),
            "w1": _w((lambda *s: torch.randn(*s, dtype=torch.bfloat16) * 0.05)(D, Dff)),
            "w2": _w((lambda *s: torch.randn(*s, dtype=torch.bfloat16) * 0.05)(Dff, D)),
        }
    _B_in = _B_setup()

    return {
        "A_x": _A_in["x"],
        "A_k_cache": _A_in["k_cache"],
        "A_v_cache": _A_in["v_cache"],
        "A_wq": _A_in["wq"],
        "A_wo": _A_in["wo"],
        "B_x": _B_in["x"],
        "B_w1": _B_in["w1"],
        "B_w2": _B_in["w2"],
        "gate_w": _w((lambda *s: torch.randn(*s, dtype=torch.bfloat16) * 0.05)(640) * 16.0),
    }

def reference(A_x, A_k_cache, A_v_cache, A_wq, A_wo, B_x, B_w1, B_w2, gate_w):
    def _A_ref(x, k_cache, v_cache, wq, wo):
        B, Sq, D = x.shape
        Hq, Dh = 32, 192
        Hkv, Sk = k_cache.shape[1], k_cache.shape[2]
        W = min(4096, max(256, Sk // 4))
        rep = Hq // Hkv
        q = (lambda x, *d: x.permute(*d))((x @ wq).reshape(B, Sq, Hq, Dh), 0, 2, 1, 3)
        k = (lambda x, n, axis: torch.repeat_interleave(x, n, dim=axis))(k_cache[:, :, Sk - W:, :], rep, axis=1)
        v = (lambda x, n, axis: torch.repeat_interleave(x, n, dim=axis))(v_cache[:, :, Sk - W:, :], rep, axis=1)
        scores = torch.einsum("bhqd,bhkd->bhqk", q, k) * (Dh ** -0.5)
        p = _softmax(scores, axis=-1)
        o = torch.einsum("bhqk,bhkd->bhqd", p, v)
        return (lambda x, *d: x.permute(*d))(o, 0, 2, 1, 3).reshape(B, Sq, Hq * Dh) @ wo

    def _B_ref(x, w1, w2):
        h = torch.einsum("bsd,df->bsf", x, w1)
        zero = torch.zeros_like(h)
        h = torch.maximum(h, zero)
        return torch.einsum("bsf,fd->bsd", h, w2)

    x = A_x
    g = _sigmoid((x * gate_w).sum(-1, keepdims=True))
    a = _A_ref(x, A_k_cache, A_v_cache, A_wq, A_wo)
    b = _B_ref(x, B_w1, B_w2)
    return g * a + (1 - g) * b
```

```cpp
#include <hip/hip_runtime.h>
#include <hip/hip_bf16.h>
#include <cstdint>

typedef unsigned short u16;
typedef unsigned v4u __attribute__((ext_vector_type(4)));
constexpr int B = 4, D = 640, H = 32, SK = 4096, DH = 192, W = 1024, DFF = 1536;
constexpr int NQ = H * DH;

__device__ __forceinline__ float bf2f(u16 v) { return __uint_as_float((unsigned)v << 16); }
__device__ __forceinline__ float lo(unsigned v) { return __uint_as_float(v << 16); }
__device__ __forceinline__ float hi(unsigned v) { return __uint_as_float(v & 0xffff0000u); }
__device__ __forceinline__ float rbf(float f) { return __bfloat162float(__float2bfloat16(f)); }

__global__ void k1(const u16* x, const u16* wq, const u16* w1, float* q, float* hbuf) {
  __shared__ float xs[B][D];
  for (int i = threadIdx.x; i < B * D; i += blockDim.x) xs[i / D][i % D] = bf2f(x[i]);
  __syncthreads();
  int n = blockIdx.x * blockDim.x + threadIdx.x;
  const u16* w; int ld; int col; bool isq = n < NQ;
  if (isq) { w = wq; ld = NQ; col = n; } else { w = w1; ld = DFF; col = n - NQ; if (col >= DFF) return; }
  float a0 = 0, a1 = 0, a2 = 0, a3 = 0;
  for (int d = 0; d < D; ++d) { float v = bf2f(w[(size_t)d * ld + col]); a0 += xs[0][d] * v; a1 += xs[1][d] * v; a2 += xs[2][d] * v; a3 += xs[3][d] * v; }
  if (isq) { q[0 * NQ + col] = rbf(a0); q[1 * NQ + col] = rbf(a1); q[2 * NQ + col] = rbf(a2); q[3 * NQ + col] = rbf(a3); }
  else { hbuf[0 * DFF + col] = fmaxf(rbf(a0), 0.f); hbuf[1 * DFF + col] = fmaxf(rbf(a1), 0.f); hbuf[2 * DFF + col] = fmaxf(rbf(a2), 0.f); hbuf[3 * DFF + col] = fmaxf(rbf(a3), 0.f); }
}

__global__ void __launch_bounds__(256) k2(const float* q, const u16* kc, const u16* vc, float* o) {
  __shared__ float qs[DH]; __shared__ float sc[W]; __shared__ float red[256];
  int bh = blockIdx.x, b = bh / H, h = bh % H, t = threadIdx.x;
  if (t < DH) qs[t] = q[b * NQ + h * DH + t];
  __syncthreads();
  const u16* kb = kc + ((size_t)bh * SK + (SK - W)) * DH;
  const u16* vb = vc + ((size_t)bh * SK + (SK - W)) * DH;
  float mx = -INFINITY;
  for (int k = t; k < W; k += 256) { float s = 0; const v4u* r = (const v4u*)(kb + (size_t)k * DH);
    for (int c = 0; c < 24; ++c) { v4u v = r[c]; for (int e = 0; e < 4; ++e) { s += lo(v[e]) * qs[c * 8 + 2 * e] + hi(v[e]) * qs[c * 8 + 2 * e + 1]; } }
    s *= 0.07216878364870322f; sc[k] = s; mx = fmaxf(mx, s); }
  red[t] = mx; __syncthreads();
  for (int s = 128; s > 0; s >>= 1) { if (t < s) red[t] = fmaxf(red[t], red[t + s]); __syncthreads(); }
  mx = red[0]; __syncthreads();
  float sum = 0;
  for (int k = t; k < W; k += 256) { float p = __expf(sc[k] - mx); sc[k] = p; sum += p; }
  red[t] = sum; __syncthreads();
  for (int s = 128; s > 0; s >>= 1) { if (t < s) red[t] += red[t + s]; __syncthreads(); }
  sum = red[0];
  if (t < DH) { float a = 0; for (int k = 0; k < W; ++k) a += sc[k] * bf2f(vb[(size_t)k * DH + t]); o[b * NQ + h * DH + t] = rbf(a / sum); }
}

__global__ void __launch_bounds__(256) k3(const u16* x, const u16* gw, const float* o, const float* hbuf, const u16* wo, const u16* w2, __hip_bfloat16* out) {
  __shared__ float pa[4][B][64], pb[4][B][64]; __shared__ float gs[B];
  int t = threadIdx.x, c = t & 63, kg = t >> 6, col = blockIdx.x * 64 + c;
  if (t < B) { float s = 0; for (int d = 0; d < D; ++d) s += bf2f(x[t * D + d]) * bf2f(gw[d]); gs[t] = 1.f / (1.f + __expf(-s)); }
  float a[B] = {0, 0, 0, 0}, bb[B] = {0, 0, 0, 0};
  for (int k = kg; k < NQ; k += 4) { float w = bf2f(wo[(size_t)k * D + col]); for (int b = 0; b < B; ++b) a[b] += o[b * NQ + k] * w; }
  for (int k = kg; k < DFF; k += 4) { float w = bf2f(w2[(size_t)k * D + col]); for (int b = 0; b < B; ++b) bb[b] += hbuf[b * DFF + k] * w; }
  for (int b = 0; b < B; ++b) { pa[kg][b][c] = a[b]; pb[kg][b][c] = bb[b]; }
  __syncthreads();
  int b = kg;
  float A = pa[0][b][c] + pa[1][b][c] + pa[2][b][c] + pa[3][b][c];
  float Bv = pb[0][b][c] + pb[1][b][c] + pb[2][b][c] + pb[3][b][c];
  float g = gs[b];
  out[b * D + col] = __float2bfloat16(g * A + (1.f - g) * Bv);
}

extern "C" void kernel_launch(void* const* d_in, const int* in_sizes, int n_in, void* d_out, int out_size, void* d_ws, size_t ws_size, hipStream_t stream) {
  const u16* x = (const u16*)d_in[0]; const u16* kc = (const u16*)d_in[1]; const u16* vc = (const u16*)d_in[2];
  const u16* wq = (const u16*)d_in[3]; const u16* wo = (const u16*)d_in[4]; const u16* w1 = (const u16*)d_in[6]; const u16* w2 = (const u16*)d_in[7]; const u16* gw = (const u16*)d_in[8];
  float* q = (float*)d_ws; float* hbuf = q + B * NQ; float* o = hbuf + B * DFF;
  k1<<<(NQ + DFF) / 256, 256, 0, stream>>>(x, wq, w1, q, hbuf);
  k2<<<B * H, 256, 0, stream>>>(q, kc, vc, o);
  k3<<<10, 256, 0, stream>>>(x, gw, o, hbuf, wo, w2, (__hip_bfloat16*)d_out);
}
```

```cpp
#include <hip/hip_runtime.h>
#include <hip/hip_bf16.h>
#include <stdint.h>

#define KV_AUX 2
#define WT_AUX 2
#define WINDOW 12
#define QPOS 23
#define MLPOS 24
#define STAMPS 0

typedef unsigned short u16;
typedef unsigned long long u64;
typedef unsigned v4u __attribute__((ext_vector_type(4)));
typedef unsigned v2u __attribute__((ext_vector_type(2)));
typedef float f32x4 __attribute__((ext_vector_type(4)));
typedef float f32x2 __attribute__((ext_vector_type(2)));
typedef __bf16 bf16x2 __attribute__((ext_vector_type(2)));
typedef __bf16 bf16x8 __attribute__((ext_vector_type(8)));
typedef short s16x4 __attribute__((ext_vector_type(4)));
#define GAS __attribute__((address_space(1)))
#define LAS __attribute__((address_space(3)))
typedef GAS unsigned gu32;
typedef GAS unsigned long long gu64;
#define RLX_AGENT __ATOMIC_RELAXED, __HIP_MEMORY_SCOPE_AGENT

constexpr int NB = 4, DM = 640, NH = 32, SK = 4096, DH = 192, WIN = 1024, DFF = 1536, NQ = NH * DH, NCOL = NQ + DFF;
constexpr int NWG = 256, NTHR = 256;
constexpr float SCL2 = 0.07216878364870322f * 1.4426950408889634f;
constexpr size_t WS_EPOCH = 0;
constexpr size_t WS_S1 = 4096;
constexpr size_t WS_ML = WS_S1 + (size_t)NB * 2 * NCOL * 8;
constexpr size_t WS_OG = WS_ML + (size_t)NB * NH * 16 * 8;
constexpr size_t WS_PG = WS_OG + (size_t)NB * 2 * NQ * 8;
constexpr size_t WS_END = WS_PG + (size_t)20 * 10 * NB * 64 * 8;
constexpr size_t WS_STAMP = 4u << 20;
constexpr unsigned SPIN_CAP = 1u << 16;

constexpr int L_WT = 0;
constexpr int L_Z = L_WT + 384 * 128;
constexpr int L_P = L_Z + 4 * 832;
constexpr int L_Q = L_P + 4 * 128 * 4;
constexpr int L_O = L_Q + 4 * 384;
constexpr int L_USED = L_O + 4 * 192 * 4;
constexpr int L_BYTES = 84 * 1024;
static_assert(L_USED <= L_BYTES, "lds");

struct Params {
  const u16* x; const u16* kc; const u16* vc; const u16* wq; const u16* wo; const u16* w1; const u16* w2; const u16* gw;
  u16* out; unsigned char* ws;
};

__device__ __forceinline__ float f_lo(unsigned v) { return __uint_as_float(v << 16); }
__device__ __forceinline__ float f_hi(unsigned v) { return __uint_as_float(v & 0xffff0000u); }
__device__ __forceinline__ float bf2f(u16 v) { return __uint_as_float((unsigned)v << 16); }
__device__ __forceinline__ float dot2(unsigned k, unsigned q, float acc) {
  return __builtin_amdgcn_fdot2_f32_bf16(__builtin_bit_cast(bf16x2, k), __builtin_bit_cast(bf16x2, q), acc, false);
}
template <int CTRL> __device__ __forceinline__ float dpp(float x) {
  return __builtin_bit_cast(float, __builtin_amdgcn_mov_dpp(__builtin_bit_cast(int, x), CTRL, 0xf, 0xf, true));
}
constexpr int XOR1 = 0xB1, XOR2 = 0x4E, XOR7 = 0x141, XOR8 = 0x128;
__device__ __forceinline__ float swap32_add(float a, float b) {
  auto t = __builtin_amdgcn_permlane32_swap(__float_as_uint(a), __float_as_uint(b), false, false);
  return __uint_as_float(t[0]) + __uint_as_float(t[1]);
}
__device__ __forceinline__ float swap16_add(float a, float b) {
  auto t = __builtin_amdgcn_permlane16_swap(__float_as_uint(a), __float_as_uint(b), false, false);
  return __uint_as_float(t[0]) + __uint_as_float(t[1]);
}
__device__ __forceinline__ float wave_max(float x) {
  x = fmaxf(x, dpp<XOR1>(x)); x = fmaxf(x, dpp<XOR2>(x)); x = fmaxf(x, dpp<XOR7>(x)); x = fmaxf(x, dpp<XOR8>(x));
  auto s = __builtin_amdgcn_permlane16_swap(__float_as_uint(x), __float_as_uint(x), false, false);
  x = fmaxf(__uint_as_float(s[0]), __uint_as_float(s[1]));
  auto t = __builtin_amdgcn_permlane32_swap(__float_as_uint(x), __float_as_uint(x), false, false);
  return fmaxf(__uint_as_float(t[0]), __uint_as_float(t[1]));
}
__device__ __forceinline__ float wave_sum(float x) {
  x += dpp<XOR1>(x); x += dpp<XOR2>(x); x += dpp<XOR7>(x); x += dpp<XOR8>(x);
  auto s = __builtin_amdgcn_permlane16_swap(__float_as_uint(x), __float_as_uint(x), false, false);
  x = __uint_as_float(s[0]) + __uint_as_float(s[1]);
  auto t = __builtin_amdgcn_permlane32_swap(__float_as_uint(x), __float_as_uint(x), false, false);
  return __uint_as_float(t[0]) + __uint_as_float(t[1]);
}
__device__ __forceinline__ unsigned f2bf(float f) { return (unsigned)__builtin_bit_cast(u16, (__bf16)f); }
__device__ __forceinline__ u64 ld_gran(gu64* g) { return __hip_atomic_load(g, RLX_AGENT); }
__device__ __forceinline__ __amdgpu_buffer_rsrc_t mk_rsrc(const void* p, unsigned bytes) {
  return __builtin_amdgcn_make_buffer_rsrc(const_cast<void*>(p), 0, bytes, 0x00020000);
}
__device__ __forceinline__ f32x4 mfma16(v4u a, v4u b, f32x4 c) {
  return __builtin_amdgcn_mfma_f32_16x16x32_bf16(__builtin_bit_cast(bf16x8, a), __builtin_bit_cast(bf16x8, b), c, 0, 0, 0);
}
typedef short v4i16_t __attribute__((ext_vector_type(4)));
__device__ __forceinline__ v2u tr_read(const unsigned char* p) {
  return __builtin_bit_cast(v2u, __builtin_amdgcn_ds_read_tr16_b64_v4i16((LAS v4i16_t*)p));
}
#define SB() __builtin_amdgcn_sched_barrier(0)
#define WAITFOR(v) do { asm volatile("" ::"v"(v)); SB(); } while (0)
#define STAMP(i) do {} while (0)

__global__ void __launch_bounds__(NTHR, 1) fused_kernel(Params p) {
  __shared__ __attribute__((aligned(16))) unsigned char smem[L_BYTES];
  const int tid = threadIdx.x, lane = tid & 63, w = __builtin_amdgcn_readfirstlane(tid >> 6), r = lane >> 3, c = lane & 7;
  const int wg = blockIdx.x, bh = wg >> 1, s = wg & 1, b = bh >> 5, h = bh & 31;
  constexpr int W = WINDOW;
  STAMP(0);
  const unsigned ep = __hip_atomic_load((gu32*)(p.ws + WS_EPOCH), RLX_AGENT);
  const bool has1 = wg >= 16;
  const int u1 = has1 ? wg - 16 : 0, lc = u1 >> 1, half = u1 & 1;
  const bool isq = lc < 96;
  const __amdgpu_buffer_rsrc_t rs1 = mk_rsrc(isq ? p.wq : p.w1, has1 ? (isq ? DM * NQ * 2 : DM * DFF * 2) : 0);
  const unsigned ld1 = isq ? NQ * 2 : DFF * 2;
  const unsigned vo1 = (320 * half + 8 * w + r) * ld1 + (isq ? lc : lc - 96) * 128 + 16 * c;
  const size_t kvoff = ((size_t)bh * SK + (SK - WIN) + 512 * s) * DH;
  const __amdgpu_buffer_rsrc_t rsK = mk_rsrc(p.kc + kvoff, 512 * DH * 2), rsV = mk_rsrc(p.vc + kvoff, 512 * DH * 2);
  const unsigned vkv = w * 3072 + r * 384 + c * 16;
  const bool has3 = wg < 200;
  const int rb = wg / 10, cb = wg - rb * 10;
  const bool iso = rb < 16;
  const __amdgpu_buffer_rsrc_t rs3 = mk_rsrc(iso ? p.wo : p.w2, has3 ? (iso ? NQ * DM * 2 : DFF * DM * 2) : 0);
  const unsigned vo3 = ((iso ? rb : rb - 16) * 384 + 8 * w + r) * (DM * 2) + cb * 128 + 16 * c;
  const __amdgpu_buffer_rsrc_t rsW = mk_rsrc(p.ws, (unsigned)WS_END);
  const __amdgpu_buffer_rsrc_t rsX = mk_rsrc(p.x, NB * DM * 2);
  v4u W1[10], K[48], V[48], W3[12], XA[10];
#define ISSUE(n_) do { const int n__ = (n_); \
    if (n__ < 10) W1[n__] = __builtin_amdgcn_raw_buffer_load_b128(rs1, vo1, n__ * 32 * ld1, WT_AUX); \
    else if (n__ < 58) K[n__ - 10] = __builtin_amdgcn_raw_buffer_load_b128(rsK, vkv + ((n__ - 10) % 3) * 128, ((n__ - 10) / 3) * 12288, KV_AUX); \
    else if (n__ < 106) V[n__ - 58] = __builtin_amdgcn_raw_buffer_load_b128(rsV, vkv + ((n__ - 58) % 3) * 128, ((n__ - 58) / 3) * 12288, KV_AUX); \
    else if (n__ < 118) W3[n__ - 106] = __builtin_amdgcn_raw_buffer_load_b128(rs3, vo3, (n__ - 106) * 32 * (DM * 2), WT_AUX); \
    SB(); } while (0)
#define WAITK(m_) WAITFOR(K[m_])
  const unsigned wt_wr = L_WT + (8 * w + r) * 128 + ((((c >> 1) ^ (((r >> 1) & 1) | ((w & 1) << 1))) << 5) | ((c & 1) << 4));
  const int g4 = lane >> 4, q4 = (lane & 15) >> 2, p4 = lane & 3;
  const unsigned wt_rd = L_WT + (8 * g4 + q4) * 128 + (((w ^ (((q4 >> 1) & 1) | ((g4 & 1) << 1))) << 5) | (p4 << 3));

#pragma unroll
  for (int n = 0; n < 10; ++n) ISSUE(n);
  {
    const unsigned xo = (q4 * DM + 320 * half + 8 * g4) * 2;
#pragma unroll
    for (int i = 0; i < 10; ++i) { XA[i] = __builtin_amdgcn_raw_buffer_load_b128(rsX, xo + 64 * i, 0, 0); SB(); }
  }
#pragma unroll
  for (int n = 10; n < W; ++n) ISSUE(n);
#pragma unroll
  for (int i = 0; i < 10; ++i) {
    *reinterpret_cast<v4u*>(smem + wt_wr + i * 4096) = W1[i];
    SB();
    ISSUE(W + i);
  }
  const unsigned tag = ep * 0x9E3779B1u + 0x7F4A7C15u;
  __syncthreads();
  STAMP(1);
  {
    f32x4 d = {0.f, 0.f, 0.f, 0.f};
#pragma unroll
    for (int ks = 0; ks < 10; ++ks) {
      const v2u t0 = tr_read(smem + wt_rd + ks * 4096), t1 = tr_read(smem + wt_rd + ks * 4096 + 512);
      d = mfma16(XA[ks], v4u{t0.x, t0.y, t1.x, t1.y}, d);
    }
    const unsigned off = has1 ? (unsigned)(WS_S1 + (size_t)((g4 * 2 + half) * NCOL + 64 * lc + 16 * w + (lane & 15)) * 8) : 0xFFFFFFF0u;
    v2u g = {__float_as_uint(d[0]), tag};
    __builtin_amdgcn_raw_buffer_store_b64(g, rsW, off, 0, 16);
    SB();
  }
  STAMP(2);
#pragma unroll
  for (int n = W + 10; n <= 10 + QPOS; ++n) { WAITK(n - W - 10); ISSUE(n); }
  STAMP(3);
  gu64* qg = (gu64*)(p.ws + WS_S1) + (size_t)b * 2 * NCOL + h * DH + lane;
  u64 qv[6];
#pragma unroll
  for (int j = 0; j < 3; ++j) { qv[2 * j] = ld_gran(qg + 64 * j); qv[2 * j + 1] = ld_gran(qg + NCOL + 64 * j); }
  SB();
#pragma unroll
  for (int n = 10 + QPOS + 1; n <= 10 + QPOS + W; ++n) { WAITK(n - W - 10); ISSUE(n); }
  {
    bool ok = true;
#pragma unroll
    for (int j = 0; j < 6; ++j) ok &= (unsigned)(qv[j] >> 32) == tag;
    unsigned spins = 0;
    if (__builtin_expect(!__all(ok), 0)) {
      do {
        __builtin_amdgcn_s_sleep(2);
#pragma unroll
        for (int j = 0; j < 3; ++j) { qv[2 * j] = ld_gran(qg + 64 * j); qv[2 * j + 1] = ld_gran(qg + NCOL + 64 * j); }
        ok = true;
#pragma unroll
        for (int j = 0; j < 6; ++j) ok &= (unsigned)(qv[j] >> 32) == tag;
      } while (!__all(ok) && ++spins < SPIN_CAP);
      ++spins;
    }
  }
  STAMP(4);
  v4u qp[3];
  {
    u16* ql = reinterpret_cast<u16*>(smem + L_Q + w * 384);
#pragma unroll
    for (int j = 0; j < 3; ++j) ql[64 * j + lane] = (u16)f2bf(__uint_as_float((unsigned)qv[2 * j]) + __uint_as_float((unsigned)qv[2 * j + 1]));
    asm volatile("s_waitcnt lgkmcnt(0)" ::: "memory");
#pragma unroll
    for (int j = 0; j < 3; ++j) qp[j] = *reinterpret_cast<const v4u*>(smem + L_Q + w * 384 + j * 128 + c * 16);
  }
  float sc[16];
#define QK_GROUP(i) do { float d_ = 0.f; \
    _Pragma("unroll") for (int j = 0; j < 3; ++j) _Pragma("unroll") for (int k = 0; k < 4; ++k) d_ = dot2(K[3 * (i) + j][k], qp[j][k], d_); \
    sc[i] = d_; asm volatile("" : "+v"(sc[i])); SB(); } while (0)
  constexpr int G0 = (QPOS + 1) / 3;
#pragma unroll
  for (int i = 0; i < G0; ++i) QK_GROUP(i);
#pragma unroll
  for (int m = QPOS + 1; m < 48; ++m) {
    if (m % 3 == 2) { QK_GROUP(m / 3); } else { WAITK(m); }
    ISSUE(10 + m + W);
  }
#define TR_STEP(BASE, HALF, CTRL, BIT) _Pragma("unroll") for (int q_ = 0; q_ < (HALF); ++q_) { \
    const float t1_ = sc[BASE + q_] + dpp<CTRL>(sc[BASE + q_]), t2_ = sc[BASE + q_ + (HALF)] + dpp<CTRL>(sc[BASE + q_ + (HALF)]); sc[BASE + q_] = (c & (BIT)) ? t2_ : t1_; }
  TR_STEP(0, 4, XOR7, 4) TR_STEP(0, 2, XOR2, 2) TR_STEP(0, 1, XOR1, 1)
  TR_STEP(8, 4, XOR7, 4) TR_STEP(8, 2, XOR2, 2) TR_STEP(8, 1, XOR1, 1)
#undef TR_STEP
  const float sa = sc[0] * SCL2, sb = sc[8] * SCL2;
  const float mw = wave_max(fmaxf(sa, sb));
  const float pa = __builtin_amdgcn_exp2f(sa - mw), pb = __builtin_amdgcn_exp2f(sb - mw);
  const float lw = wave_sum(pa + pb);
  {
    v2u g = {__float_as_uint(lane == 0 ? mw : lw), tag};
    const unsigned off = lane < 2 ? (unsigned)(WS_ML + (size_t)(bh * 16 + (s * 4 + w) * 2 + lane) * 8) : 0xFFFFFFF0u;
    __builtin_amdgcn_raw_buffer_store_b64(g, rsW, off, 0, 16);
  }
  *reinterpret_cast<float*>(smem + L_P + (w * 128 + lane) * 4) = pa;
  *reinterpret_cast<float*>(smem + L_P + (w * 128 + 64 + lane) * 4) = pb;
  asm volatile("s_waitcnt lgkmcnt(0)" ::: "memory");
  f32x4 pv[4];
  pv[0] = *reinterpret_cast<const f32x4*>(smem + L_P + (w * 128 + 8 * r) * 4);
  pv[1] = *reinterpret_cast<const f32x4*>(smem + L_P + (w * 128 + 8 * r + 4) * 4);
  pv[2] = *reinterpret_cast<const f32x4*>(smem + L_P + (w * 128 + 64 + 8 * r) * 4);
  pv[3] = *reinterpret_cast<const f32x4*>(smem + L_P + (w * 128 + 64 + 8 * r + 4) * 4);
  STAMP(5);
  gu64* mlg = (gu64*)(p.ws + WS_ML) + (size_t)bh * 16 + (lane & 15);
  u64 mlv = 0;
  float fac = 0.f;
  f32x2 acc[3][4];
#pragma unroll
  for (int j = 0; j < 3; ++j)
#pragma unroll
    for (int k = 0; k < 4; ++k) acc[j][k] = f32x2{0.f, 0.f};
#pragma unroll
  for (int m = 0; m < 48; ++m) {
    const int i = m / 3, j = m % 3;
    const float pi = pv[i >> 2][i & 3];
    const f32x2 pp = {pi, pi};
#pragma unroll
    for (int k = 0; k < 4; ++k) acc[j][k] = __builtin_elementwise_fma(pp, f32x2{f_lo(V[m][k]), f_hi(V[m][k])}, acc[j][k]);
    asm volatile("" : "+v"(acc[j][0]), "+v"(acc[j][1]), "+v"(acc[j][2]), "+v"(acc[j][3]));
    SB();
    ISSUE(58 + m + W);
    if (m + W == MLPOS) { mlv = ld_gran(mlg); SB(); }
    if (m == MLPOS + 1) {
      if (__builtin_expect(!__all((unsigned)(mlv >> 32) == tag), 0)) {
        unsigned spins = 0;
        do {
          __builtin_amdgcn_s_sleep(1);
          mlv = ld_gran(mlg);
        } while (!__all((unsigned)(mlv >> 32) == tag) && ++spins < SPIN_CAP);
      }
      const float val = __uint_as_float((unsigned)mlv);
      const bool odd = lane & 1;
      const float M = wave_max(odd ? -INFINITY : val);
      const float mp = dpp<XOR1>(val);
      float t = (odd && lane < 16) ? __builtin_amdgcn_exp2f(mp - M) * val : 0.f;
      const float L = wave_sum(t);
      fac = __builtin_amdgcn_exp2f(mw - M) / L;
      asm volatile("" : "+v"(fac));
      SB();
    }
  }
  STAMP(6);
  {
    float o3[3];
#pragma unroll
    for (int j = 0; j < 3; ++j) {
      float a4[4], a2[2];
#pragma unroll
      for (int e = 0; e < 4; ++e) a4[e] = swap32_add(acc[j][e >> 1][e & 1], acc[j][(e + 4) >> 1][e & 1]);
#pragma unroll
      for (int e = 0; e < 2; ++e) a2[e] = swap16_add(a4[e], a4[e + 2]);
      const float t1 = a2[0] + dpp<XOR8>(a2[0]), t2 = a2[1] + dpp<XOR8>(a2[1]);
      o3[j] = ((lane & 8) ? t2 : t1) * fac;
    }
#pragma unroll
    for (int j = 0; j < 3; ++j) *reinterpret_cast<float*>(smem + L_O + (w * 192 + 64 * j + 8 * c + r) * 4) = o3[j];
  }
  __syncthreads();
  {
    const int t = tid < 192 ? tid : 191;
    float a = 0.f;
#pragma unroll
    for (int ww = 0; ww < 4; ++ww) a += *reinterpret_cast<const float*>(smem + L_O + (ww * 192 + t) * 4);
    const unsigned off = tid < 192 ? (unsigned)(WS_OG + (size_t)((b * 2 + s) * NQ + h * DH + tid) * 8) : 0xFFFFFFF0u;
    v2u g = {__float_as_uint(a), tag};
    __builtin_amdgcn_raw_buffer_store_b64(g, rsW, off, 0, 16);
    SB();
  }
  STAMP(7);
  if (has3) {
    gu64* src = iso ? (gu64*)(p.ws + WS_OG) + rb * 384 : (gu64*)(p.ws + WS_S1) + NQ + (rb - 16) * 384;
    const int pitch = iso ? NQ : NCOL;
    u64 zv[12];
    int zo[6];
#pragma unroll
    for (int k = 0; k < 6; ++k) { const int pi = tid + 256 * k, bb = pi / 384, row = pi - bb * 384; zo[k] = bb * 2 * pitch + row; }
#pragma unroll
    for (int k = 0; k < 6; ++k) { zv[2 * k] = ld_gran(src + zo[k]); zv[2 * k + 1] = ld_gran(src + zo[k] + pitch); }
    SB();
#pragma unroll
    for (int i = 0; i < 12; ++i) *reinterpret_cast<v4u*>(smem + wt_wr + i * 4096) = W3[i];
    __syncthreads();
    v4u bf[12];
#pragma unroll
    for (int ks = 0; ks < 12; ++ks) {
      const v2u t0 = tr_read(smem + wt_rd + ks * 4096), t1 = tr_read(smem + wt_rd + ks * 4096 + 512);
      bf[ks] = v4u{t0.x, t0.y, t1.x, t1.y};
    }
    {
      bool ok = true;
#pragma unroll
      for (int k = 0; k < 12; ++k) ok &= (unsigned)(zv[k] >> 32) == tag;
      if (!__all(ok)) {
        unsigned spins = 0;
        do {
#pragma unroll
          for (int k = 0; k < 6; ++k) { zv[2 * k] = ld_gran(src + zo[k]); zv[2 * k + 1] = ld_gran(src + zo[k] + pitch); }
          ok = true;
#pragma unroll
          for (int k = 0; k < 12; ++k) ok &= (unsigned)(zv[k] >> 32) == tag;
        } while (!__all(ok) && ++spins < SPIN_CAP);
      }
    }
    STAMP(8);
#pragma unroll
    for (int k = 0; k < 6; ++k) {
      const int pi = tid + 256 * k, bb = pi / 384, row = pi - bb * 384;
      float z = __uint_as_float((unsigned)zv[2 * k]) + __uint_as_float((unsigned)zv[2 * k + 1]);
      if (!iso) z = fmaxf(z, 0.f);
      *reinterpret_cast<u16*>(smem + L_Z + bb * 832 + row * 2) = (u16)f2bf(z);
    }
    __syncthreads();
    f32x4 d = {0.f, 0.f, 0.f, 0.f};
#pragma unroll
    for (int ks = 0; ks < 12; ++ks) {
      const v4u a = *reinterpret_cast<const v4u*>(smem + L_Z + q4 * 832 + (32 * ks + 8 * g4) * 2);
      d = mfma16(a, bf[ks], d);
    }
    {
      v2u g = {__float_as_uint(d[0]), tag};
      __builtin_amdgcn_raw_buffer_store_b64(g, rsW, (unsigned)(WS_PG + (size_t)((rb * 10 + cb) * 256 + g4 * 64 + 16 * w + (lane & 15)) * 8), 0, 16);
    }
    STAMP(9);
  } else if (wg < 240 && w < 2) {
    const int item = (wg - 200) * 2 + w, fcb = item >> 3, fb = (item >> 1) & 3, fh = item & 1;
    float gs = 0.f;
#pragma unroll
    for (int k = 0; k < 10; ++k) gs = fmaf(bf2f(p.x[fb * DM + 64 * k + lane]), bf2f(p.gw[64 * k + lane]), gs);
    gs = wave_sum(gs);
    const float g = 1.f / (1.f + __expf(-gs));
    gu64* src = (gu64*)(p.ws + WS_PG) + ((size_t)(lane >> 5) * 10 + fcb) * 256 + fb * 64 + fh * 32 + (lane & 31);
    u64 pv2[10];
    unsigned spins = 0;
    for (;;) {
      bool ok = true;
#pragma unroll
      for (int i = 0; i < 10; ++i) pv2[i] = ld_gran(src + (size_t)i * 2 * 10 * 256);
#pragma unroll
      for (int i = 0; i < 10; ++i) ok &= (unsigned)(pv2[i] >> 32) == tag;
      if (__all(ok)) break;
      if (++spins > SPIN_CAP) break;
    }
    float A = 0.f, Bv = 0.f;
#pragma unroll
    for (int i = 0; i < 8; ++i) A += __uint_as_float((unsigned)pv2[i]);
#pragma unroll
    for (int i = 8; i < 10; ++i) Bv += __uint_as_float((unsigned)pv2[i]);
    float res = g * A + (1.f - g) * Bv;
    auto t = __builtin_amdgcn_permlane32_swap(__float_as_uint(res), __float_as_uint(res), false, false);
    res = __uint_as_float(t[0]) + __uint_as_float(t[1]);
    if (lane < 32) p.out[fb * DM + fcb * 64 + fh * 32 + lane] = (u16)f2bf(res);
    if (item == 0 && lane == 0) __hip_atomic_store((gu32*)(p.ws + WS_EPOCH), ep + 1u, RLX_AGENT);
    STAMP(10);
  }
}

extern "C" void kernel_launch(void* const* d_in, const int* in_sizes, int n_in, void* d_out, int out_size, void* d_ws, size_t ws_size, hipStream_t stream) {
  Params p;
  p.x = (const u16*)d_in[0]; p.kc = (const u16*)d_in[1]; p.vc = (const u16*)d_in[2]; p.wq = (const u16*)d_in[3]; p.wo = (const u16*)d_in[4];
  p.w1 = (const u16*)d_in[6]; p.w2 = (const u16*)d_in[7]; p.gw = (const u16*)d_in[8]; p.out = (u16*)d_out; p.ws = (unsigned char*)d_ws;
  hipLaunchKernelGGL(fused_kernel, dim3(NWG), dim3(NTHR), 0, stream, p);
}
```

```cpp
#include <hip/hip_runtime.h>
#include <hip/hip_bf16.h>
#include <stdint.h>

typedef unsigned short u16;
typedef unsigned long long u64;
typedef unsigned v4u __attribute__((ext_vector_type(4)));
typedef unsigned v2u __attribute__((ext_vector_type(2)));
typedef float f32x4 __attribute__((ext_vector_type(4)));
typedef float f32x2 __attribute__((ext_vector_type(2)));
typedef __bf16 bf16x2 __attribute__((ext_vector_type(2)));
typedef __bf16 bf16x8 __attribute__((ext_vector_type(8)));
typedef short v4i16_t __attribute__((ext_vector_type(4)));
#define GAS __attribute__((address_space(1)))
#define LAS __attribute__((address_space(3)))
typedef GAS unsigned gu32;
typedef GAS unsigned long long gu64;
#define RLX_AGENT __ATOMIC_RELAXED, __HIP_MEMORY_SCOPE_AGENT

constexpr int NB = 4, DM = 640, NH = 32, SK = 4096, DH = 192, WIN = 1024, DFF = 1536, NQ = NH * DH, NCOL = NQ + DFF;
constexpr int NWG = 256, NTHR = 256;
constexpr float SCL2 = 0.07216878364870322f * 1.4426950408889634f;
constexpr int WINDOW = 16;
constexpr int QPOS = 23;
constexpr int MLPOS = 32;
constexpr int ZPOS = 8;
constexpr int PACE = 240;
constexpr int PACE_HEAD = -40, PACE_FROM = WINDOW + 10, PACE_EVERY = 3, TB_RD = 6;
constexpr int AUX_NT = 2, AUX_SC1 = 16;
constexpr unsigned CODE_PF_OFF = 11264, CODE_PF_LEN = 5760;
constexpr size_t WS_EPOCH = 0;
constexpr size_t WS_T0 = 256;
constexpr size_t WS_S1 = 4096;
constexpr size_t WS_ML = WS_S1 + (size_t)NB * 2 * NCOL * 8;
constexpr size_t WS_OG = WS_ML + (size_t)NB * NH * 16 * 8;
constexpr size_t WS_PG = WS_OG + (size_t)NB * 2 * (NQ / 2) * 8;
constexpr size_t WS_END = WS_PG + (size_t)20 * 10 * NB * 64 * 8;
constexpr unsigned SPIN_CAP = 1u << 16;
constexpr unsigned OOB = 0xFFFFFFF0u;
constexpr int L_WT = 0;
constexpr int L_Z = L_WT + 384 * 128;
constexpr int L_P = L_Z + 4 * 832;
constexpr int L_Q = L_P + 4 * 128 * 4;
constexpr int L_O = L_Q + 4 * 384;
constexpr int L_USED = L_O + 4 * 192 * 4;
constexpr int L_BYTES = 84 * 1024;
static_assert(L_USED <= L_BYTES, "lds");

struct Params {
  const u16* x; const u16* kc; const u16* vc; const u16* wq; const u16* wo; const u16* w1; const u16* w2; const u16* gw;
  u16* out; unsigned char* ws;
};

__device__ __forceinline__ float f_lo(unsigned v) { return __uint_as_float(v << 16); }
__device__ __forceinline__ float f_hi(unsigned v) { return __uint_as_float(v & 0xffff0000u); }
__device__ __forceinline__ float bf2f(u16 v) { return __uint_as_float((unsigned)v << 16); }
__device__ __forceinline__ float dot2(unsigned k, unsigned q, float acc) {
  return __builtin_amdgcn_fdot2_f32_bf16(__builtin_bit_cast(bf16x2, k), __builtin_bit_cast(bf16x2, q), acc, false);
}
template <int CTRL> __device__ __forceinline__ float dpp(float x) {
  return __builtin_bit_cast(float, __builtin_amdgcn_mov_dpp(__builtin_bit_cast(int, x), CTRL, 0xf, 0xf, true));
}
constexpr int XOR1 = 0xB1, XOR2 = 0x4E, XOR7 = 0x141, XOR8 = 0x128;
__device__ __forceinline__ float swap32_add(float a, float b) {
  auto t = __builtin_amdgcn_permlane32_swap(__float_as_uint(a), __float_as_uint(b), false, false);
  return __uint_as_float(t[0]) + __uint_as_float(t[1]);
}
__device__ __forceinline__ float swap16_add(float a, float b) {
  auto t = __builtin_amdgcn_permlane16_swap(__float_as_uint(a), __float_as_uint(b), false, false);
  return __uint_as_float(t[0]) + __uint_as_float(t[1]);
}
__device__ __forceinline__ float wave_max(float x) {
  x = fmaxf(x, dpp<XOR1>(x)); x = fmaxf(x, dpp<XOR2>(x)); x = fmaxf(x, dpp<XOR7>(x)); x = fmaxf(x, dpp<XOR8>(x));
  auto s = __builtin_amdgcn_permlane16_swap(__float_as_uint(x), __float_as_uint(x), false, false);
  x = fmaxf(__uint_as_float(s[0]), __uint_as_float(s[1]));
  auto t = __builtin_amdgcn_permlane32_swap(__float_as_uint(x), __float_as_uint(x), false, false);
  return fmaxf(__uint_as_float(t[0]), __uint_as_float(t[1]));
}
__device__ __forceinline__ float wave_sum(float x) {
  x += dpp<XOR1>(x); x += dpp<XOR2>(x); x += dpp<XOR7>(x); x += dpp<XOR8>(x);
  auto s = __builtin_amdgcn_permlane16_swap(__float_as_uint(x), __float_as_uint(x), false, false);
  x = __uint_as_float(s[0]) + __uint_as_float(s[1]);
  auto t = __builtin_amdgcn_permlane32_swap(__float_as_uint(x), __float_as_uint(x), false, false);
  return __uint_as_float(t[0]) + __uint_as_float(t[1]);
}
__device__ __forceinline__ unsigned f2bf(float f) { return (unsigned)__builtin_bit_cast(u16, (__bf16)f); }
__device__ __forceinline__ unsigned pk2bf(float lo, float hi) { bf16x2 v = {(__bf16)lo, (__bf16)hi}; return __builtin_bit_cast(unsigned, v); }
__device__ __forceinline__ u64 ld_gran(gu64* g) { return __hip_atomic_load(g, RLX_AGENT); }
__device__ __forceinline__ __amdgpu_buffer_rsrc_t mk_rsrc(const void* p, unsigned bytes) {
  return __builtin_amdgcn_make_buffer_rsrc(const_cast<void*>(p), 0, bytes, 0x00020000);
}
__device__ __forceinline__ f32x4 mfma16(v4u a, v4u b, f32x4 c) {
  return __builtin_amdgcn_mfma_f32_16x16x32_bf16(__builtin_bit_cast(bf16x8, a), __builtin_bit_cast(bf16x8, b), c, 0, 0, 0);
}
__device__ __forceinline__ v2u tr_read(const unsigned char* p) {
  return __builtin_bit_cast(v2u, __builtin_amdgcn_ds_read_tr16_b64_v4i16((LAS v4i16_t*)p));
}
#define SB() __builtin_amdgcn_sched_barrier(0)
#define WAITFOR(v) do { asm volatile("" ::"v"(v)); SB(); } while (0)

__global__ void __launch_bounds__(NTHR, 1) gated_mix_decode_kernel(Params p) {
  __shared__ __attribute__((aligned(16))) unsigned char smem[L_BYTES];
  const int tid = threadIdx.x, lane = tid & 63, w = __builtin_amdgcn_readfirstlane(tid >> 6), r = lane >> 3, c = lane & 7;
  const int wg = blockIdx.x, bh = (wg & 7) * 16 + (wg >> 4), s = (wg >> 3) & 1, b = bh >> 5, h = bh & 31;
  constexpr int W = WINDOW;
  unsigned tzero = (unsigned)__builtin_amdgcn_s_memrealtime();
  u64 pc0;
  asm volatile("s_getpc_b64 %0" : "=s"(pc0));
  unsigned zoff = 0;
  asm volatile("" : "+v"(zoff));
  const unsigned ep = __hip_atomic_load((gu32*)(p.ws + WS_EPOCH + zoff), RLX_AGENT);
  const bool has1 = wg >= 16;
  const int u1 = has1 ? wg - 16 : 0, lc = u1 >> 1, half = u1 & 1;
  const bool isq = lc < 96;
  const __amdgpu_buffer_rsrc_t rs1 = mk_rsrc(isq ? p.wq : p.w1, has1 ? (isq ? DM * NQ * 2 : DM * DFF * 2) : 0);
  const unsigned ld1 = isq ? NQ * 2 : DFF * 2;
  const unsigned vo1 = (320 * half + 8 * w + r) * ld1 + (isq ? lc : lc - 96) * 128 + 16 * c;
  const size_t kvoff = ((size_t)bh * SK + (SK - WIN) + 512 * s) * DH;
  const __amdgpu_buffer_rsrc_t rsK = mk_rsrc(p.kc + kvoff, 512 * DH * 2), rsV = mk_rsrc(p.vc + kvoff, 512 * DH * 2);
  const unsigned vkv = w * 3072 + r * 384 + c * 16;
  const bool has3 = wg < 200;
  const int rb = wg / 10, cb = wg - rb * 10;
  const bool iso = rb < 16;
  const __amdgpu_buffer_rsrc_t rs3 = mk_rsrc(iso ? p.wo : p.w2, has3 ? (iso ? NQ * DM * 2 : DFF * DM * 2) : 0);
  const unsigned vo3 = ((iso ? rb : rb - 16) * 384 + 8 * w + r) * (DM * 2) + cb * 128 + 16 * c;
  const __amdgpu_buffer_rsrc_t rsW = mk_rsrc(p.ws, (unsigned)WS_END);
  const __amdgpu_buffer_rsrc_t rsX = mk_rsrc(p.x, NB * DM * 2);
  v4u W1[10], K[48], V[48], W3[12], XA[10];
#define ISSUE(n_) do { const int n__ = (n_); \
    if (n__ >= PACE_FROM && n__ < 118 && (n__ % PACE_EVERY) == 0) { \
      const unsigned tgt_ = tzero + (unsigned)(PACE_HEAD + ((n__ * PACE) >> 4)); \
      while ((int)((unsigned)__builtin_amdgcn_s_memrealtime() - tgt_) < 0) __builtin_amdgcn_s_sleep(1); \
      SB(); } \
    if (n__ < 10) W1[n__] = __builtin_amdgcn_raw_buffer_load_b128(rs1, vo1, n__ * 32 * ld1, AUX_NT); \
    else if (n__ < 58) K[n__ - 10] = __builtin_amdgcn_raw_buffer_load_b128(rsK, vkv + ((n__ - 10) % 3) * 128, ((n__ - 10) / 3) * 12288, AUX_NT); \
    else if (n__ < 106) V[n__ - 58] = __builtin_amdgcn_raw_buffer_load_b128(rsV, vkv + ((n__ - 58) % 3) * 128, ((n__ - 58) / 3) * 12288, AUX_NT); \
    else if (n__ < 118) W3[n__ - 106] = __builtin_amdgcn_raw_buffer_load_b128(rs3, vo3, (n__ - 106) * 32 * (DM * 2), AUX_NT); \
    SB(); } while (0)
  const unsigned wt_wr = L_WT + (8 * w + r) * 128 + ((((c >> 1) ^ (((r >> 1) & 1) | ((w & 1) << 1))) << 5) | ((c & 1) << 4));
  const int g4 = lane >> 4, q4 = (lane & 15) >> 2, p4 = lane & 3;
  const unsigned wt_rd = L_WT + (8 * g4 + q4) * 128 + (((w ^ (((q4 >> 1) & 1) | ((g4 & 1) << 1))) << 5) | (p4 << 3));

#pragma unroll
  for (int n = 0; n < 10; ++n) ISSUE(n);
  {
    const unsigned xo = (q4 * DM + 320 * half + 8 * g4) * 2;
#pragma unroll
    for (int i = 0; i < 10; ++i) { XA[i] = __builtin_amdgcn_raw_buffer_load_b128(rsX, xo + 64 * i, 0, 0); SB(); }
  }
#pragma unroll
  for (int n = 10; n < W; ++n) ISSUE(n);
#pragma unroll
  for (int i = 0; i < 10; ++i) {
    *reinterpret_cast<v4u*>(smem + wt_wr + i * 4096) = W1[i];
    SB();
    ISSUE(W + i);
  }
  const unsigned tag = ep * 0x9E3779B1u + 0x7F4A7C15u;
  __syncthreads();
  {
    f32x4 d = {0.f, 0.f, 0.f, 0.f};
#pragma unroll
    for (int ks = 0; ks < 10; ++ks) {
      const v2u t0 = tr_read(smem + wt_rd + ks * 4096), t1 = tr_read(smem + wt_rd + ks * 4096 + 512);
      d = mfma16(XA[ks], v4u{t0.x, t0.y, t1.x, t1.y}, d);
    }
    const unsigned off = has1 ? (unsigned)(WS_S1 + (size_t)((g4 * 2 + half) * NCOL + 64 * lc + 16 * w + (lane & 15)) * 8) : OOB;
    __builtin_amdgcn_raw_buffer_store_b64(v2u{__float_as_uint(d[0]), tag}, rsW, off, 0, AUX_SC1);
    __builtin_amdgcn_raw_buffer_store_b64(v2u{tzero, tag}, rsW, (wg == 0 && tid == 0) ? (unsigned)WS_T0 : OOB, 0, AUX_SC1);
    SB();
  }
  u64 t0g = 0;
#pragma unroll
  for (int n = W + 10; n <= 10 + QPOS; ++n) {
    WAITFOR(K[n - W - 10]); ISSUE(n);
    if (n == W + 10 + TB_RD) { t0g = ld_gran((gu64*)(p.ws + WS_T0)); SB(); }
  }
  gu64* qg = (gu64*)(p.ws + WS_S1) + (size_t)b * 2 * NCOL + h * DH + lane;
  u64 qv[6];
#pragma unroll
  for (int j = 0; j < 3; ++j) { qv[2 * j] = ld_gran(qg + 64 * j); qv[2 * j + 1] = ld_gran(qg + NCOL + 64 * j); }
  SB();
#pragma unroll
  for (int n = 10 + QPOS + 1; n <= 10 + QPOS + W; ++n) { WAITFOR(K[n - W - 10]); ISSUE(n); }
  {
    bool ok = true;
#pragma unroll
    for (int j = 0; j < 6; ++j) ok &= (unsigned)(qv[j] >> 32) == tag;
    if (__builtin_expect(!__all(ok), 0)) {
      unsigned spins = 0;
      do {
        __builtin_amdgcn_s_sleep(2);
#pragma unroll
        for (int j = 0; j < 3; ++j) { qv[2 * j] = ld_gran(qg + 64 * j); qv[2 * j + 1] = ld_gran(qg + NCOL + 64 * j); }
        ok = true;
#pragma unroll
        for (int j = 0; j < 6; ++j) ok &= (unsigned)(qv[j] >> 32) == tag;
      } while (!__all(ok) && ++spins < SPIN_CAP);
    }
  }
  {
    const unsigned tv = __builtin_amdgcn_readfirstlane((unsigned)t0g), tg = __builtin_amdgcn_readfirstlane((unsigned)(t0g >> 32));
    const int d = (int)(tzero - tv);
    if (tg == (unsigned)__builtin_amdgcn_readfirstlane(tag) && d > 0 && d < 300) tzero = tv;
  }
  v4u qp[3];
  {
    u16* ql = reinterpret_cast<u16*>(smem + L_Q + w * 384);
#pragma unroll
    for (int j = 0; j < 3; ++j) ql[64 * j + lane] = (u16)f2bf(__uint_as_float((unsigned)qv[2 * j]) + __uint_as_float((unsigned)qv[2 * j + 1]));
    asm volatile("s_waitcnt lgkmcnt(0)" ::: "memory");
#pragma unroll
    for (int j = 0; j < 3; ++j) qp[j] = *reinterpret_cast<const v4u*>(smem + L_Q + w * 384 + j * 128 + c * 16);
  }
  float sc[16];
#define QK_GROUP(i) do { float d_ = 0.f; \
    _Pragma("unroll") for (int j = 0; j < 3; ++j) _Pragma("unroll") for (int k = 0; k < 4; ++k) d_ = dot2(K[3 * (i) + j][k], qp[j][k], d_); \
    sc[i] = d_; asm volatile("" : "+v"(sc[i])); SB(); } while (0)
  constexpr int G0 = (QPOS + 1) / 3;
#pragma unroll
  for (int i = 0; i < G0; ++i) QK_GROUP(i);
#pragma unroll
  for (int m = 3 * G0; m < 48; ++m) {
    if (m % 3 == 2) { QK_GROUP(m / 3); } else if (m > QPOS) { WAITFOR(K[m]); }
    if (m > QPOS) ISSUE(10 + m + W);
  }
#define TR_STEP(BASE, HALF, CTRL, BIT) _Pragma("unroll") for (int q_ = 0; q_ < (HALF); ++q_) { \
    const float t1_ = sc[BASE + q_] + dpp<CTRL>(sc[BASE + q_]), t2_ = sc[BASE + q_ + (HALF)] + dpp<CTRL>(sc[BASE + q_ + (HALF)]); sc[BASE + q_] = (c & (BIT)) ? t2_ : t1_; }
  TR_STEP(0, 4, XOR7, 4) TR_STEP(0, 2, XOR2, 2) TR_STEP(0, 1, XOR1, 1)
  TR_STEP(8, 4, XOR7, 4) TR_STEP(8, 2, XOR2, 2) TR_STEP(8, 1, XOR1, 1)
#undef TR_STEP
  const float sa = sc[0] * SCL2, sb = sc[8] * SCL2;
  const float mw = wave_max(fmaxf(sa, sb));
  const float pa = __builtin_amdgcn_exp2f(sa - mw), pb = __builtin_amdgcn_exp2f(sb - mw);
  const float lw = wave_sum(pa + pb);
  {
    const unsigned off = lane < 2 ? (unsigned)(WS_ML + (size_t)(bh * 16 + (s * 4 + w) * 2 + lane) * 8) : OOB;
    __builtin_amdgcn_raw_buffer_store_b64(v2u{__float_as_uint(lane == 0 ? mw : lw), tag}, rsW, off, 0, AUX_SC1);
  }
  *reinterpret_cast<float*>(smem + L_P + (w * 128 + lane) * 4) = pa;
  *reinterpret_cast<float*>(smem + L_P + (w * 128 + 64 + lane) * 4) = pb;
  asm volatile("s_waitcnt lgkmcnt(0)" ::: "memory");
  f32x4 pv[4];
  pv[0] = *reinterpret_cast<const f32x4*>(smem + L_P + (w * 128 + 8 * r) * 4);
  pv[1] = *reinterpret_cast<const f32x4*>(smem + L_P + (w * 128 + 8 * r + 4) * 4);
  pv[2] = *reinterpret_cast<const f32x4*>(smem + L_P + (w * 128 + 64 + 8 * r) * 4);
  pv[3] = *reinterpret_cast<const f32x4*>(smem + L_P + (w * 128 + 64 + 8 * r + 4) * 4);
  gu64* mlg = (gu64*)(p.ws + WS_ML) + (size_t)bh * 16 + (lane & 15);
  u64 mlv = 0;
  unsigned cpf = 0;
  float fac = 0.f;
  f32x2 acc[3][4];
#pragma unroll
  for (int j = 0; j < 3; ++j)
#pragma unroll
    for (int k = 0; k < 4; ++k) acc[j][k] = f32x2{0.f, 0.f};
#pragma unroll
  for (int m = 0; m < 48; ++m) {
    const int i = m / 3, j = m % 3;
    const float pi = pv[i >> 2][i & 3];
    const f32x2 pp = {pi, pi};
#pragma unroll
    for (int k = 0; k < 4; ++k) acc[j][k] = __builtin_elementwise_fma(pp, f32x2{f_lo(V[m][k]), f_hi(V[m][k])}, acc[j][k]);
    asm volatile("" : "+v"(acc[j][0]), "+v"(acc[j][1]), "+v"(acc[j][2]), "+v"(acc[j][3]));
    SB();
    ISSUE(58 + m + W);
    if (m + W == MLPOS) {
      mlv = ld_gran(mlg);
      cpf = __builtin_amdgcn_raw_buffer_load_b32(mk_rsrc((const void*)(pc0 + CODE_PF_OFF), w == 0 ? CODE_PF_LEN : 0), lane * 128, 0, 0);
      SB();
    }
    if (m == MLPOS + 1) {
      if (__builtin_expect(!__all((unsigned)(mlv >> 32) == tag), 0)) {
        unsigned spins = 0;
        do {
          __builtin_amdgcn_s_sleep(1);
          mlv = ld_gran(mlg);
        } while (!__all((unsigned)(mlv >> 32) == tag) && ++spins < SPIN_CAP);
      }
      const float val = __uint_as_float((unsigned)mlv);
      const bool odd = lane & 1;
      const float M = wave_max(odd ? -INFINITY : val);
      const float mp = dpp<XOR1>(val);
      const float t = (odd && lane < 16) ? __builtin_amdgcn_exp2f(mp - M) * val : 0.f;
      const float L = wave_sum(t);
      fac = __builtin_amdgcn_exp2f(mw - M) / L;
      asm volatile("" : "+v"(fac));
      asm volatile("" ::"v"(cpf));
      SB();
    }
    if (m >= 45) {
      float a4[4], a2[2];
#pragma unroll
      for (int e = 0; e < 4; ++e) a4[e] = swap32_add(acc[j][e >> 1][e & 1], acc[j][(e + 4) >> 1][e & 1]);
#pragma unroll
      for (int e = 0; e < 2; ++e) a2[e] = swap16_add(a4[e], a4[e + 2]);
      const float t1 = a2[0] + dpp<XOR8>(a2[0]), t2 = a2[1] + dpp<XOR8>(a2[1]);
      *reinterpret_cast<float*>(smem + L_O + (w * 192 + 64 * j + 8 * c + r) * 4) = ((lane & 8) ? t2 : t1) * fac;
      SB();
    }
  }
#pragma unroll
  for (int n = 58 + 48 + W; n < 118; ++n) ISSUE(n);
  __syncthreads();
  {
    const int t = tid < 96 ? tid : 95;
    f32x2 a = {0.f, 0.f};
#pragma unroll
    for (int ww = 0; ww < 4; ++ww) a += *reinterpret_cast<const f32x2*>(smem + L_O + (ww * 192 + 2 * t) * 4);
    const unsigned off = tid < 96 ? (unsigned)(WS_OG + (size_t)((b * 2 + s) * (NQ / 2) + h * (DH / 2) + tid) * 8) : OOB;
    __builtin_amdgcn_raw_buffer_store_b64(v2u{pk2bf(a.x, a.y), tag}, rsW, off, 0, AUX_SC1);
    SB();
  }
  if (__builtin_expect(has3, 1)) {
    v4u bf[12], af[12];
    const unsigned pgo = (unsigned)(WS_PG + (size_t)((rb * 10 + cb) * 256 + g4 * 64 + 16 * w + (lane & 15)) * 8);
    if (__builtin_expect(iso, 1)) {
      const int i1 = tid < 128 ? tid + 256 : tid, b0 = tid / 96, k0 = tid - b0 * 96, b1 = i1 / 96, k1 = i1 - b1 * 96;
      const unsigned zo0 = (unsigned)(WS_OG + (size_t)(b0 * 2 * (NQ / 2) + rb * 192 + 2 * k0) * 8);
      const unsigned zo1 = (unsigned)(WS_OG + (size_t)(b1 * 2 * (NQ / 2) + rb * 192 + 2 * k1) * 8);
      const unsigned zl0 = L_Z + b0 * 832 + k0 * 8, zl1 = L_Z + b1 * 832 + k1 * 8;
      v4u A[4], B[4];
#define ZISSUE(S) do { asm volatile("" ::: "memory");   \
        S[0] = __builtin_amdgcn_raw_buffer_load_b128(rsW, zo0, 0, AUX_SC1); S[1] = __builtin_amdgcn_raw_buffer_load_b128(rsW, zo0, (NQ / 2) * 8, AUX_SC1); \
        S[2] = __builtin_amdgcn_raw_buffer_load_b128(rsW, zo1, 0, AUX_SC1); S[3] = __builtin_amdgcn_raw_buffer_load_b128(rsW, zo1, (NQ / 2) * 8, AUX_SC1); SB(); } while (0)
#define ZOK(S) __all((((S[0].y ^ tag) | (S[0].w ^ tag) | (S[1].y ^ tag)) | ((S[1].w ^ tag) | (S[2].y ^ tag) | (S[2].w ^ tag)) | ((S[3].y ^ tag) | (S[3].w ^ tag))) == 0u)
#define ZPUT(S) do { \
        v2u z0 = {pk2bf(f_lo(S[0].x) + f_lo(S[1].x), f_hi(S[0].x) + f_hi(S[1].x)), pk2bf(f_lo(S[0].z) + f_lo(S[1].z), f_hi(S[0].z) + f_hi(S[1].z))}; \
        *reinterpret_cast<v2u*>(smem + zl0) = z0; \
        v2u z1 = {pk2bf(f_lo(S[2].x) + f_lo(S[3].x), f_hi(S[2].x) + f_hi(S[3].x)), pk2bf(f_lo(S[2].z) + f_lo(S[3].z), f_hi(S[2].z) + f_hi(S[3].z))}; \
        *reinterpret_cast<v2u*>(smem + zl1) = z1; SB(); } while (0)
#pragma unroll
      for (int i = 0; i < ZPOS; ++i) { *reinterpret_cast<v4u*>(smem + wt_wr + i * 4096) = W3[i]; SB(); }
      ZISSUE(A);
#pragma unroll
      for (int i = ZPOS; i < 12; ++i) { *reinterpret_cast<v4u*>(smem + wt_wr + i * 4096) = W3[i]; SB(); }
      ZISSUE(B);
      __syncthreads();
#pragma unroll
      for (int ks = 0; ks < 12; ++ks) {
        const v2u t0 = tr_read(smem + wt_rd + ks * 4096), t1 = tr_read(smem + wt_rd + ks * 4096 + 512);
        bf[ks] = v4u{t0.x, t0.y, t1.x, t1.y};
      }
      SB();
      if (__builtin_expect(ZOK(A), 1)) { ZPUT(A); }
      else if (__builtin_expect(ZOK(B), 1)) { ZPUT(B); }
      else {
        v4u C[4];
        unsigned spins = 0;
        do { ZISSUE(C); } while (!ZOK(C) && ++spins < SPIN_CAP);
        ZPUT(C);
      }
      __syncthreads();
#pragma unroll
      for (int ks = 0; ks < 12; ++ks) af[ks] = *reinterpret_cast<const v4u*>(smem + L_Z + q4 * 832 + (32 * ks + 8 * g4) * 2);
      SB();
      f32x4 d = {0.f, 0.f, 0.f, 0.f}, d2 = {0.f, 0.f, 0.f, 0.f};
#pragma unroll
      for (int ks = 0; ks < 12; ks += 2) { d = mfma16(af[ks], bf[ks], d); d2 = mfma16(af[ks + 1], bf[ks + 1], d2); }
      __builtin_amdgcn_raw_buffer_store_b64(v2u{__float_as_uint(d[0] + d2[0]), tag}, rsW, pgo, 0, AUX_SC1);
      SB();
#pragma unroll
      for (int k = 0; k < 4; ++k) { asm volatile("" ::"v"(A[k])); asm volatile("" ::"v"(B[k])); }
    } else {
      gu64* src = (gu64*)(p.ws + WS_S1) + NQ + (rb - 16) * 384;
      u64 zv[12];
      int zo[6];
#pragma unroll
      for (int k = 0; k < 6; ++k) { const int pi = tid + 256 * k, bb = pi / 384, row = pi - bb * 384; zo[k] = bb * 2 * NCOL + row; }
#pragma unroll
      for (int k = 0; k < 6; ++k) { zv[2 * k] = ld_gran(src + zo[k]); zv[2 * k + 1] = ld_gran(src + zo[k] + NCOL); }
      SB();
#pragma unroll
      for (int i = 0; i < 12; ++i) *reinterpret_cast<v4u*>(smem + wt_wr + i * 4096) = W3[i];
      __syncthreads();
#pragma unroll
      for (int ks = 0; ks < 12; ++ks) {
        const v2u t0 = tr_read(smem + wt_rd + ks * 4096), t1 = tr_read(smem + wt_rd + ks * 4096 + 512);
        bf[ks] = v4u{t0.x, t0.y, t1.x, t1.y};
      }
      SB();
      bool ok = true;
#pragma unroll
      for (int k = 0; k < 12; ++k) ok &= (unsigned)(zv[k] >> 32) == tag;
      if (__builtin_expect(!__all(ok), 0)) {
        unsigned spins = 0;
        do {
#pragma unroll
          for (int k = 0; k < 6; ++k) { zv[2 * k] = ld_gran(src + zo[k]); zv[2 * k + 1] = ld_gran(src + zo[k] + NCOL); }
          ok = true;
#pragma unroll
          for (int k = 0; k < 12; ++k) ok &= (unsigned)(zv[k] >> 32) == tag;
        } while (!__all(ok) && ++spins < SPIN_CAP);
      }
#pragma unroll
      for (int k = 0; k < 6; ++k) {
        const int pi = tid + 256 * k, bb = pi / 384, row = pi - bb * 384;
        const float z = fmaxf(__uint_as_float((unsigned)zv[2 * k]) + __uint_as_float((unsigned)zv[2 * k + 1]), 0.f);
        *reinterpret_cast<u16*>(smem + L_Z + bb * 832 + row * 2) = (u16)f2bf(z);
      }
      __syncthreads();
#pragma unroll
      for (int ks = 0; ks < 12; ++ks) af[ks] = *reinterpret_cast<const v4u*>(smem + L_Z + q4 * 832 + (32 * ks + 8 * g4) * 2);
      SB();
      f32x4 d = {0.f, 0.f, 0.f, 0.f}, d2 = {0.f, 0.f, 0.f, 0.f};
#pragma unroll
      for (int ks = 0; ks < 12; ks += 2) { d = mfma16(af[ks], bf[ks], d); d2 = mfma16(af[ks + 1], bf[ks + 1], d2); }
      __builtin_amdgcn_raw_buffer_store_b64(v2u{__float_as_uint(d[0] + d2[0]), tag}, rsW, pgo, 0, AUX_SC1);
    }
  } else if (wg < 240) {
    const int item = (wg - 200) * 4 + w, fcb = item >> 4, fb = (item >> 2) & 3, fq = item & 3;
    float gs = 0.f;
#pragma unroll
    for (int k = 0; k < 10; ++k) gs = fmaf(bf2f(p.x[fb * DM + 64 * k + lane]), bf2f(p.gw[64 * k + lane]), gs);
    gs = wave_sum(gs);
    float g = 1.f / (1.f + __expf(-gs));
    asm volatile("" : "+v"(g));
    const unsigned po = (unsigned)(WS_PG + (size_t)((g4 * 10 + fcb) * 256 + fb * 64 + fq * 16 + (lane & 15)) * 8);
    v2u S[5];
    {
      unsigned spins = 0;
      bool ok;
      do {
        asm volatile("" ::: "memory");
#pragma unroll
        for (int i = 0; i < 5; ++i) S[i] = __builtin_amdgcn_raw_buffer_load_b64(rsW, po, i * (4 * 10 * 256 * 8), AUX_SC1);
        ok = __all((((S[0].y ^ tag) | (S[1].y ^ tag) | (S[2].y ^ tag)) | ((S[3].y ^ tag) | (S[4].y ^ tag))) == 0u);
      } while (!ok && ++spins < SPIN_CAP);
    }
    float res = g * ((__uint_as_float(S[0].x) + __uint_as_float(S[1].x)) + (__uint_as_float(S[2].x) + __uint_as_float(S[3].x))) + (1.f - g) * __uint_as_float(S[4].x);
    {
      auto t16 = __builtin_amdgcn_permlane16_swap(__float_as_uint(res), __float_as_uint(res), false, false);
      res = __uint_as_float(t16[0]) + __uint_as_float(t16[1]);
      auto t32 = __builtin_amdgcn_permlane32_swap(__float_as_uint(res), __float_as_uint(res), false, false);
      res = __uint_as_float(t32[0]) + __uint_as_float(t32[1]);
    }
    if (lane < 16) p.out[fb * DM + fcb * 64 + fq * 16 + lane] = (u16)f2bf(res);
    if (item == 0 && lane == 0) __hip_atomic_store((gu32*)(p.ws + WS_EPOCH), ep + 1u, RLX_AGENT);
  }
}

extern "C" void kernel_launch(void* const* d_in, const int* in_sizes, int n_in, void* d_out, int out_size, void* d_ws, size_t ws_size, hipStream_t stream) {
  Params p;
  p.x = (const u16*)d_in[0]; p.kc = (const u16*)d_in[1]; p.vc = (const u16*)d_in[2]; p.wq = (const u16*)d_in[3]; p.wo = (const u16*)d_in[4];
  p.w1 = (const u16*)d_in[6]; p.w2 = (const u16*)d_in[7]; p.gw = (const u16*)d_in[8]; p.out = (u16*)d_out; p.ws = (unsigned char*)d_ws;
  hipLaunchKernelGGL(gated_mix_decode_kernel, dim3(NWG), dim3(NTHR), 0, stream, p);
}
```

```cpp
#include <hip/hip_runtime.h>
#include <hip/hip_bf16.h>
#include <stdint.h>

typedef unsigned short u16;
typedef unsigned long long u64;
typedef unsigned v4u __attribute__((ext_vector_type(4)));
typedef unsigned v2u __attribute__((ext_vector_type(2)));
typedef float f32x4 __attribute__((ext_vector_type(4)));
typedef float f32x2 __attribute__((ext_vector_type(2)));
typedef __bf16 bf16x2 __attribute__((ext_vector_type(2)));
typedef __bf16 bf16x8 __attribute__((ext_vector_type(8)));
typedef short v4i16_t __attribute__((ext_vector_type(4)));
#define GAS __attribute__((address_space(1)))
#define LAS __attribute__((address_space(3)))
typedef GAS unsigned gu32;
typedef GAS unsigned long long gu64;
#define RLX_AGENT __ATOMIC_RELAXED, __HIP_MEMORY_SCOPE_AGENT

constexpr int NB = 4, DM = 640, NH = 32, SK = 4096, DH = 192, WIN = 1024, DFF = 1536, NQ = NH * DH, NCOL = NQ + DFF;
constexpr int NWG = 256, NTHR = 256;
constexpr float SCL2 = 0.07216878364870322f * 1.4426950408889634f;
constexpr int WINDOW = 16;
constexpr int QPOS = 23;
constexpr int MLPOS = 32;
constexpr int ZPOS = 8;
constexpr int PACE = 236;
constexpr int PACE_HEAD = -70, PACE_FROM = WINDOW + 10, PACE_EVERY = 3, PACE_PHASE = 2, TB_RD = 6;
constexpr int AUX_NT = 2, AUX_SC1 = 16;
constexpr unsigned CODE_PF_OFF = 11520, CODE_PF_LEN = 6656;
constexpr size_t WS_EPOCH = 0;
constexpr size_t WS_T0 = 256;
constexpr size_t WS_S1 = 4096;
constexpr size_t WS_ML = WS_S1 + (size_t)NB * 4 * NCOL * 8;
constexpr size_t WS_OG = WS_ML + (size_t)NB * NH * 16 * 8;
constexpr size_t WS_PG = WS_OG + (size_t)NB * 2 * (NQ / 2) * 8;
constexpr size_t WS_END = WS_PG + (size_t)20 * 10 * NB * 64 * 8;
constexpr unsigned SPIN_CAP = 1u << 16;
constexpr unsigned OOB = 0xFFFFFFF0u;
constexpr int L_WT = 0;
constexpr int L_Z = L_WT + 384 * 128;
constexpr int L_P = L_Z + 4 * 832;
constexpr int L_Q = L_P + 4 * 128 * 4;
constexpr int L_O = L_Q + 4 * 384;
constexpr int L_USED = L_O + 4 * 192 * 4;
constexpr int L_BYTES = 84 * 1024;
static_assert(L_USED <= L_BYTES, "lds");

struct Params {
  const u16* x; const u16* kc; const u16* vc; const u16* wq; const u16* wo; const u16* w1; const u16* w2; const u16* gw;
  u16* out; unsigned char* ws;
};

__device__ __forceinline__ float f_lo(unsigned v) { return __uint_as_float(v << 16); }
__device__ __forceinline__ float f_hi(unsigned v) { return __uint_as_float(v & 0xffff0000u); }
__device__ __forceinline__ float bf2f(u16 v) { return __uint_as_float((unsigned)v << 16); }
__device__ __forceinline__ float dot2(unsigned k, unsigned q, float acc) {
  return __builtin_amdgcn_fdot2_f32_bf16(__builtin_bit_cast(bf16x2, k), __builtin_bit_cast(bf16x2, q), acc, false);
}
template <int CTRL> __device__ __forceinline__ float dpp(float x) {
  return __builtin_bit_cast(float, __builtin_amdgcn_mov_dpp(__builtin_bit_cast(int, x), CTRL, 0xf, 0xf, true));
}
constexpr int XOR1 = 0xB1, XOR2 = 0x4E, XOR7 = 0x141, XOR8 = 0x128;
__device__ __forceinline__ float swap32_add(float a, float b) {
  auto t = __builtin_amdgcn_permlane32_swap(__float_as_uint(a), __float_as_uint(b), false, false);
  return __uint_as_float(t[0]) + __uint_as_float(t[1]);
}
__device__ __forceinline__ float swap16_add(float a, float b) {
  auto t = __builtin_amdgcn_permlane16_swap(__float_as_uint(a), __float_as_uint(b), false, false);
  return __uint_as_float(t[0]) + __uint_as_float(t[1]);
}
__device__ __forceinline__ float wave_max(float x) {
  x = fmaxf(x, dpp<XOR1>(x)); x = fmaxf(x, dpp<XOR2>(x)); x = fmaxf(x, dpp<XOR7>(x)); x = fmaxf(x, dpp<XOR8>(x));
  auto s = __builtin_amdgcn_permlane16_swap(__float_as_uint(x), __float_as_uint(x), false, false);
  x = fmaxf(__uint_as_float(s[0]), __uint_as_float(s[1]));
  auto t = __builtin_amdgcn_permlane32_swap(__float_as_uint(x), __float_as_uint(x), false, false);
  return fmaxf(__uint_as_float(t[0]), __uint_as_float(t[1]));
}
__device__ __forceinline__ float wave_sum(float x) {
  x += dpp<XOR1>(x); x += dpp<XOR2>(x); x += dpp<XOR7>(x); x += dpp<XOR8>(x);
  auto s = __builtin_amdgcn_permlane16_swap(__float_as_uint(x), __float_as_uint(x), false, false);
  x = __uint_as_float(s[0]) + __uint_as_float(s[1]);
  auto t = __builtin_amdgcn_permlane32_swap(__float_as_uint(x), __float_as_uint(x), false, false);
  return __uint_as_float(t[0]) + __uint_as_float(t[1]);
}
__device__ __forceinline__ unsigned f2bf(float f) { return (unsigned)__builtin_bit_cast(u16, (__bf16)f); }
__device__ __forceinline__ unsigned pk2bf(float lo, float hi) { bf16x2 v = {(__bf16)lo, (__bf16)hi}; return __builtin_bit_cast(unsigned, v); }
__device__ __forceinline__ u64 ld_gran(gu64* g) { return __hip_atomic_load(g, RLX_AGENT); }
__device__ __forceinline__ __amdgpu_buffer_rsrc_t mk_rsrc(const void* p, unsigned bytes) {
  return __builtin_amdgcn_make_buffer_rsrc(const_cast<void*>(p), 0, bytes, 0x00020000);
}
__device__ __forceinline__ f32x4 mfma16(v4u a, v4u b, f32x4 c) {
  return __builtin_amdgcn_mfma_f32_16x16x32_bf16(__builtin_bit_cast(bf16x8, a), __builtin_bit_cast(bf16x8, b), c, 0, 0, 0);
}
__device__ __forceinline__ v2u tr_read(const unsigned char* p) {
  return __builtin_bit_cast(v2u, __builtin_amdgcn_ds_read_tr16_b64_v4i16((LAS v4i16_t*)p));
}
#define SB() __builtin_amdgcn_sched_barrier(0)
#define WAITFOR(v) do { asm volatile("" ::"v"(v)); SB(); } while (0)

__global__ void __launch_bounds__(NTHR, 1) gated_mix_decode_kernel(Params p) {
  __shared__ __attribute__((aligned(16))) unsigned char smem[L_BYTES];
  const int tid = threadIdx.x, lane = tid & 63, w = __builtin_amdgcn_readfirstlane(tid >> 6), r = lane >> 3, c = lane & 7;
  const int wg = blockIdx.x, bh = (wg & 7) * 16 + (wg >> 4), s = (wg >> 3) & 1, b = bh >> 5, h = bh & 31;
  constexpr int W = WINDOW;
  unsigned tzero = (unsigned)__builtin_amdgcn_s_memrealtime();
  const unsigned td0 = tzero;
  u64 pc0;
  asm volatile("s_getpc_b64 %0" : "=s"(pc0));
  unsigned zoff = 0;
  asm volatile("" : "+v"(zoff));
  const unsigned ep = __hip_atomic_load((gu32*)(p.ws + WS_EPOCH + zoff), RLX_AGENT);
  const bool has1 = wg >= 16;
  const int u1 = has1 ? wg - 16 : 0, cbk = u1 >> 2, qtr = u1 & 3;
  const bool isq = cbk < 48;
  const __amdgpu_buffer_rsrc_t rs1 = mk_rsrc(isq ? p.wq : p.w1, has1 ? (isq ? DM * NQ * 2 : DM * DFF * 2) : 0);
  const unsigned ld1 = isq ? NQ * 2 : DFF * 2;
  const unsigned vo1 = (160 * qtr + 4 * w + (lane >> 4)) * ld1 + (isq ? cbk : cbk - 48) * 256 + 16 * (lane & 15);
  const size_t kvoff = ((size_t)bh * SK + (SK - WIN) + 512 * s) * DH;
  const __amdgpu_buffer_rsrc_t rsK = mk_rsrc(p.kc + kvoff, 512 * DH * 2), rsV = mk_rsrc(p.vc + kvoff, 512 * DH * 2);
  const unsigned vkv = w * 3072 + r * 384 + c * 16;
  const bool has3 = wg < 200;
  const int rb = wg / 10, cb = wg - rb * 10;
  const bool iso = rb < 16;
  const __amdgpu_buffer_rsrc_t rs3 = mk_rsrc(iso ? p.wo : p.w2, has3 ? (iso ? NQ * DM * 2 : DFF * DM * 2) : 0);
  const unsigned vo3 = ((iso ? rb : rb - 16) * 384 + 8 * w + r) * (DM * 2) + cb * 128 + 16 * c;
  const __amdgpu_buffer_rsrc_t rsW = mk_rsrc(p.ws, (unsigned)WS_END);
  const __amdgpu_buffer_rsrc_t rsX = mk_rsrc(p.x, NB * DM * 2);
  v4u W1[10], K[48], V[48], W3[12], XA[5];
#define ISSUE(n_) do { const int n__ = (n_); \
    if (n__ >= PACE_FROM && n__ < 118 && ((n__ + PACE_PHASE) % PACE_EVERY) == 0) { \
      const unsigned tgt_ = tzero + (unsigned)(PACE_HEAD + ((n__ * PACE) >> 4)); \
      while ((int)((unsigned)__builtin_amdgcn_s_memrealtime() - tgt_) < 0) __builtin_amdgcn_s_sleep(1); \
      SB(); } \
    if (n__ < 10) W1[n__] = __builtin_amdgcn_raw_buffer_load_b128(rs1, vo1, n__ * 16 * ld1, AUX_NT); \
    else if (n__ < 58) K[n__ - 10] = __builtin_amdgcn_raw_buffer_load_b128(rsK, vkv + ((n__ - 10) % 3) * 128, ((n__ - 10) / 3) * 12288, AUX_NT); \
    else if (n__ < 106) V[n__ - 58] = __builtin_amdgcn_raw_buffer_load_b128(rsV, vkv + ((n__ - 58) % 3) * 128, ((n__ - 58) / 3) * 12288, AUX_NT); \
    else if (n__ < 118) W3[n__ - 106] = __builtin_amdgcn_raw_buffer_load_b128(rs3, vo3, (n__ - 106) * 32 * (DM * 2), AUX_NT); \
    SB(); } while (0)
  const unsigned wt_wr = L_WT + (8 * w + r) * 128 + ((((c >> 1) ^ (((r >> 1) & 1) | ((w & 1) << 1))) << 5) | ((c & 1) << 4));
  const int g4 = lane >> 4, q4 = (lane & 15) >> 2, p4 = lane & 3;
  const unsigned wt_rd = L_WT + (8 * g4 + q4) * 128 + (((w ^ (((q4 >> 1) & 1) | ((g4 & 1) << 1))) << 5) | (p4 << 3));

  const unsigned w1_wr = L_WT + (4 * w + g4) * 256 + (((((lane & 15) >> 1) ^ (g4 | ((w >> 1) << 2))) << 5) | ((lane & 1) << 4));
  const unsigned w1_rd = L_WT + (8 * g4 + q4) * 256 + ((((2 * w) ^ (q4 | ((g4 & 1) << 2))) << 5) | (p4 << 3));

#pragma unroll
  for (int n = 0; n < 10; ++n) ISSUE(n);
  {
    const unsigned xo = (q4 * DM + 160 * qtr + 8 * g4) * 2;
#pragma unroll
    for (int i = 0; i < 5; ++i) { XA[i] = __builtin_amdgcn_raw_buffer_load_b128(rsX, xo + 64 * i, 0, 0); SB(); }
  }
#pragma unroll
  for (int n = 10; n < W; ++n) ISSUE(n);
#pragma unroll
  for (int i = 0; i < 10; ++i) {
    *reinterpret_cast<v4u*>(smem + w1_wr + i * 4096) = W1[i];
    SB();
    ISSUE(W + i);
  }
  const unsigned tag = ep * 0x9E3779B1u + 0x7F4A7C15u;
  __syncthreads();
  {
    f32x4 d0 = {0.f, 0.f, 0.f, 0.f}, d1 = {0.f, 0.f, 0.f, 0.f};
#pragma unroll
    for (int ks = 0; ks < 5; ++ks) {
      const v2u t0 = tr_read(smem + w1_rd + ks * 8192), t1 = tr_read(smem + w1_rd + ks * 8192 + 1024);
      const v2u u0 = tr_read(smem + (w1_rd ^ 32) + ks * 8192), u1_ = tr_read(smem + (w1_rd ^ 32) + ks * 8192 + 1024);
      d0 = mfma16(XA[ks], v4u{t0.x, t0.y, t1.x, t1.y}, d0);
      d1 = mfma16(XA[ks], v4u{u0.x, u0.y, u1_.x, u1_.y}, d1);
    }
    const unsigned off = has1 ? (unsigned)(WS_S1 + (size_t)((g4 * 4 + qtr) * NCOL + 128 * cbk + 32 * w + (lane & 15)) * 8) : OOB;
    __builtin_amdgcn_raw_buffer_store_b64(v2u{__float_as_uint(d0[0]), tag}, rsW, off, 0, AUX_SC1);
    __builtin_amdgcn_raw_buffer_store_b64(v2u{__float_as_uint(d1[0]), tag}, rsW, off, 128, AUX_SC1);
    __builtin_amdgcn_raw_buffer_store_b64(v2u{tzero, tag}, rsW, (wg == 0 && tid == 0) ? (unsigned)WS_T0 : OOB, 0, AUX_SC1);
    SB();
  }
  u64 t0g = 0;
#pragma unroll
  for (int n = W + 10; n <= 10 + QPOS; ++n) {
    WAITFOR(K[n - W - 10]); ISSUE(n);
    if (n == W + 10 + TB_RD) { t0g = ld_gran((gu64*)(p.ws + WS_T0)); SB(); }
  }
  gu64* qg = (gu64*)(p.ws + WS_S1) + (size_t)b * 4 * NCOL + h * DH + lane;
  u64 qv[12];
#pragma unroll
  for (int j = 0; j < 3; ++j)
#pragma unroll
    for (int k = 0; k < 4; ++k) qv[4 * j + k] = ld_gran(qg + k * NCOL + 64 * j);
  SB();
#pragma unroll
  for (int n = 10 + QPOS + 1; n <= 10 + QPOS + W; ++n) { WAITFOR(K[n - W - 10]); ISSUE(n); }
  {
    bool ok = true;
#pragma unroll
    for (int j = 0; j < 12; ++j) ok &= (unsigned)(qv[j] >> 32) == tag;
    if (__builtin_expect(!__all(ok), 0)) {
      unsigned spins = 0;
      do {
        __builtin_amdgcn_s_sleep(2);
#pragma unroll
        for (int j = 0; j < 3; ++j)
#pragma unroll
          for (int k = 0; k < 4; ++k) qv[4 * j + k] = ld_gran(qg + k * NCOL + 64 * j);
        ok = true;
#pragma unroll
        for (int j = 0; j < 12; ++j) ok &= (unsigned)(qv[j] >> 32) == tag;
      } while (!__all(ok) && ++spins < SPIN_CAP);
    }
  }
  {
    const unsigned tv = __builtin_amdgcn_readfirstlane((unsigned)t0g), tg = __builtin_amdgcn_readfirstlane((unsigned)(t0g >> 32));
    const int d = (int)(tzero - tv);
    if (tg == (unsigned)__builtin_amdgcn_readfirstlane(tag) && d > 0 && d < 300) tzero = tv;
  }
  v4u qp[3];
  {
    u16* ql = reinterpret_cast<u16*>(smem + L_Q + w * 384);
#pragma unroll
    for (int j = 0; j < 3; ++j) ql[64 * j + lane] = (u16)f2bf((__uint_as_float((unsigned)qv[4 * j]) + __uint_as_float((unsigned)qv[4 * j + 1])) + (__uint_as_float((unsigned)qv[4 * j + 2]) + __uint_as_float((unsigned)qv[4 * j + 3])));
    asm volatile("s_waitcnt lgkmcnt(0)" ::: "memory");
#pragma unroll
    for (int j = 0; j < 3; ++j) qp[j] = *reinterpret_cast<const v4u*>(smem + L_Q + w * 384 + j * 128 + c * 16);
  }
  float sc[16];
#define QK_GROUP(i) do { float d_ = 0.f; \
    _Pragma("unroll") for (int j = 0; j < 3; ++j) _Pragma("unroll") for (int k = 0; k < 4; ++k) d_ = dot2(K[3 * (i) + j][k], qp[j][k], d_); \
    sc[i] = d_; asm volatile("" : "+v"(sc[i])); SB(); } while (0)
  constexpr int G0 = (QPOS + 1) / 3;
#pragma unroll
  for (int i = 0; i < G0; ++i) QK_GROUP(i);
#pragma unroll
  for (int m = 3 * G0; m < 48; ++m) {
    if (m % 3 == 2) { QK_GROUP(m / 3); } else if (m > QPOS) { WAITFOR(K[m]); }
    if (m > QPOS) ISSUE(10 + m + W);
  }
#define TR_STEP(BASE, HALF, CTRL, BIT) _Pragma("unroll") for (int q_ = 0; q_ < (HALF); ++q_) { \
    const float t1_ = sc[BASE + q_] + dpp<CTRL>(sc[BASE + q_]), t2_ = sc[BASE + q_ + (HALF)] + dpp<CTRL>(sc[BASE + q_ + (HALF)]); sc[BASE + q_] = (c & (BIT)) ? t2_ : t1_; }
  TR_STEP(0, 4, XOR7, 4) TR_STEP(0, 2, XOR2, 2) TR_STEP(0, 1, XOR1, 1)
  TR_STEP(8, 4, XOR7, 4) TR_STEP(8, 2, XOR2, 2) TR_STEP(8, 1, XOR1, 1)
#undef TR_STEP
  const float sa = sc[0] * SCL2, sb = sc[8] * SCL2;
  const float mw = wave_max(fmaxf(sa, sb));
  const float pa = __builtin_amdgcn_exp2f(sa - mw), pb = __builtin_amdgcn_exp2f(sb - mw);
  const float lw = wave_sum(pa + pb);
  {
    const unsigned off = lane < 2 ? (unsigned)(WS_ML + (size_t)(bh * 16 + (s * 4 + w) * 2 + lane) * 8) : OOB;
    __builtin_amdgcn_raw_buffer_store_b64(v2u{__float_as_uint(lane == 0 ? mw : lw), tag}, rsW, off, 0, AUX_SC1);
  }
  *reinterpret_cast<float*>(smem + L_P + (w * 128 + lane) * 4) = pa;
  *reinterpret_cast<float*>(smem + L_P + (w * 128 + 64 + lane) * 4) = pb;
  asm volatile("s_waitcnt lgkmcnt(0)" ::: "memory");
  f32x4 pv[4];
  pv[0] = *reinterpret_cast<const f32x4*>(smem + L_P + (w * 128 + 8 * r) * 4);
  pv[1] = *reinterpret_cast<const f32x4*>(smem + L_P + (w * 128 + 8 * r + 4) * 4);
  pv[2] = *reinterpret_cast<const f32x4*>(smem + L_P + (w * 128 + 64 + 8 * r) * 4);
  pv[3] = *reinterpret_cast<const f32x4*>(smem + L_P + (w * 128 + 64 + 8 * r + 4) * 4);
  gu64* mlg = (gu64*)(p.ws + WS_ML) + (size_t)bh * 16 + (lane & 15);
  u64 mlv = 0;
  unsigned cpf = 0;
  float fac = 0.f;
  f32x2 acc[3][4];
#pragma unroll
  for (int j = 0; j < 3; ++j)
#pragma unroll
    for (int k = 0; k < 4; ++k) acc[j][k] = f32x2{0.f, 0.f};
#pragma unroll
  for (int m = 0; m < 48; ++m) {
    const int i = m / 3, j = m % 3;
    const float pi = pv[i >> 2][i & 3];
    const f32x2 pp = {pi, pi};
#pragma unroll
    for (int k = 0; k < 4; ++k) acc[j][k] = __builtin_elementwise_fma(pp, f32x2{f_lo(V[m][k]), f_hi(V[m][k])}, acc[j][k]);
    asm volatile("" : "+v"(acc[j][0]), "+v"(acc[j][1]), "+v"(acc[j][2]), "+v"(acc[j][3]));
    SB();
    ISSUE(58 + m + W);
    if (m + W == MLPOS) {
      mlv = ld_gran(mlg);
      cpf = __builtin_amdgcn_raw_buffer_load_b32(mk_rsrc((const void*)(pc0 + CODE_PF_OFF), w == 0 ? CODE_PF_LEN : 0), lane * 128, 0, 0);
      SB();
    }
    if (m == MLPOS + 1) {
      if (__builtin_expect(!__all((unsigned)(mlv >> 32) == tag), 0)) {
        unsigned spins = 0;
        do {
          __builtin_amdgcn_s_sleep(1);
          mlv = ld_gran(mlg);
        } while (!__all((unsigned)(mlv >> 32) == tag) && ++spins < SPIN_CAP);
      }
      const float val = __uint_as_float((unsigned)mlv);
      const bool odd = lane & 1;
      const float M = wave_max(odd ? -INFINITY : val);
      const float mp = dpp<XOR1>(val);
      const float t = (odd && lane < 16) ? __builtin_amdgcn_exp2f(mp - M) * val : 0.f;
      const float L = wave_sum(t);
      fac = __builtin_amdgcn_exp2f(mw - M) / L;
      asm volatile("" : "+v"(fac));
      asm volatile("" ::"v"(cpf));
      SB();
    }
    if (m >= 45) {
      float a4[4], a2[2];
#pragma unroll
      for (int e = 0; e < 4; ++e) a4[e] = swap32_add(acc[j][e >> 1][e & 1], acc[j][(e + 4) >> 1][e & 1]);
#pragma unroll
      for (int e = 0; e < 2; ++e) a2[e] = swap16_add(a4[e], a4[e + 2]);
      const float t1 = a2[0] + dpp<XOR8>(a2[0]), t2 = a2[1] + dpp<XOR8>(a2[1]);
      *reinterpret_cast<float*>(smem + L_O + (w * 192 + 64 * j + 8 * c + r) * 4) = ((lane & 8) ? t2 : t1) * fac;
      SB();
    }
  }
#pragma unroll
  for (int n = 58 + 48 + W; n < 118; ++n) ISSUE(n);
  __syncthreads();
  {
    const int t = tid < 96 ? tid : 95;
    f32x2 a = {0.f, 0.f};
#pragma unroll
    for (int ww = 0; ww < 4; ++ww) a += *reinterpret_cast<const f32x2*>(smem + L_O + (ww * 192 + 2 * t) * 4);
    const unsigned off = tid < 96 ? (unsigned)(WS_OG + (size_t)((b * 2 + s) * (NQ / 2) + h * (DH / 2) + tid) * 8) : OOB;
    __builtin_amdgcn_raw_buffer_store_b64(v2u{pk2bf(a.x, a.y), tag}, rsW, off, 0, AUX_SC1);
    SB();
  }
  if (__builtin_expect(has3, 1)) {
    v4u bf[12], af[12];
    const unsigned pgo = (unsigned)(WS_PG + (size_t)((rb * 10 + cb) * 256 + g4 * 64 + 16 * w + (lane & 15)) * 8);
    if (__builtin_expect(iso, 1)) {
      const int i1 = tid < 128 ? tid + 256 : tid, b0 = tid / 96, k0 = tid - b0 * 96, b1 = i1 / 96, k1 = i1 - b1 * 96;
      const unsigned zo0 = (unsigned)(WS_OG + (size_t)(b0 * 2 * (NQ / 2) + rb * 192 + 2 * k0) * 8);
      const unsigned zo1 = (unsigned)(WS_OG + (size_t)(b1 * 2 * (NQ / 2) + rb * 192 + 2 * k1) * 8);
      const unsigned zl0 = L_Z + b0 * 832 + k0 * 8, zl1 = L_Z + b1 * 832 + k1 * 8;
      v4u A[4], B[4];
#define ZISSUE(S) do { asm volatile("" ::: "memory");   \
        S[0] = __builtin_amdgcn_raw_buffer_load_b128(rsW, zo0, 0, AUX_SC1); S[1] = __builtin_amdgcn_raw_buffer_load_b128(rsW, zo0, (NQ / 2) * 8, AUX_SC1); \
        S[2] = __builtin_amdgcn_raw_buffer_load_b128(rsW, zo1, 0, AUX_SC1); S[3] = __builtin_amdgcn_raw_buffer_load_b128(rsW, zo1, (NQ / 2) * 8, AUX_SC1); SB(); } while (0)
#define ZOK(S) __all((((S[0].y ^ tag) | (S[0].w ^ tag) | (S[1].y ^ tag)) | ((S[1].w ^ tag) | (S[2].y ^ tag) | (S[2].w ^ tag)) | ((S[3].y ^ tag) | (S[3].w ^ tag))) == 0u)
#define ZPUT(S) do { \
        v2u z0 = {pk2bf(f_lo(S[0].x) + f_lo(S[1].x), f_hi(S[0].x) + f_hi(S[1].x)), pk2bf(f_lo(S[0].z) + f_lo(S[1].z), f_hi(S[0].z) + f_hi(S[1].z))}; \
        *reinterpret_cast<v2u*>(smem + zl0) = z0; \
        v2u z1 = {pk2bf(f_lo(S[2].x) + f_lo(S[3].x), f_hi(S[2].x) + f_hi(S[3].x)), pk2bf(f_lo(S[2].z) + f_lo(S[3].z), f_hi(S[2].z) + f_hi(S[3].z))}; \
        *reinterpret_cast<v2u*>(smem + zl1) = z1; SB(); } while (0)
#pragma unroll
      for (int i = 0; i < ZPOS; ++i) { *reinterpret_cast<v4u*>(smem + wt_wr + i * 4096) = W3[i]; SB(); }
      ZISSUE(A);
#pragma unroll
      for (int i = ZPOS; i < 12; ++i) { *reinterpret_cast<v4u*>(smem + wt_wr + i * 4096) = W3[i]; SB(); }
      ZISSUE(B);
      __syncthreads();
#pragma unroll
      for (int ks = 0; ks < 12; ++ks) {
        const v2u t0 = tr_read(smem + wt_rd + ks * 4096), t1 = tr_read(smem + wt_rd + ks * 4096 + 512);
        bf[ks] = v4u{t0.x, t0.y, t1.x, t1.y};
      }
      SB();
      if (__builtin_expect(ZOK(A), 1)) { ZPUT(A); }
      else if (__builtin_expect(ZOK(B), 1)) { ZPUT(B); }
      else {
        v4u C[4];
        unsigned spins = 0;
        do { ZISSUE(C); } while (!ZOK(C) && ++spins < SPIN_CAP);
        ZPUT(C);
      }
      __syncthreads();
#pragma unroll
      for (int ks = 0; ks < 12; ++ks) af[ks] = *reinterpret_cast<const v4u*>(smem + L_Z + q4 * 832 + (32 * ks + 8 * g4) * 2);
      SB();
      f32x4 d = {0.f, 0.f, 0.f, 0.f}, d2 = {0.f, 0.f, 0.f, 0.f};
#pragma unroll
      for (int ks = 0; ks < 12; ks += 2) { d = mfma16(af[ks], bf[ks], d); d2 = mfma16(af[ks + 1], bf[ks + 1], d2); }
      __builtin_amdgcn_raw_buffer_store_b64(v2u{__float_as_uint(d[0] + d2[0]), tag}, rsW, pgo, 0, AUX_SC1);
      SB();
#pragma unroll
      for (int k = 0; k < 4; ++k) { asm volatile("" ::"v"(A[k])); asm volatile("" ::"v"(B[k])); }
    } else {
      gu64* src = (gu64*)(p.ws + WS_S1) + NQ + (rb - 16) * 384;
      u64 zv[24];
      int zo[6];
#pragma unroll
      for (int k = 0; k < 6; ++k) { const int pi = tid + 256 * k, bb = pi / 384, row = pi - bb * 384; zo[k] = bb * 4 * NCOL + row; }
#pragma unroll
      for (int k = 0; k < 6; ++k)
#pragma unroll
        for (int e = 0; e < 4; ++e) zv[4 * k + e] = ld_gran(src + zo[k] + e * NCOL);
      SB();
#pragma unroll
      for (int i = 0; i < 12; ++i) *reinterpret_cast<v4u*>(smem + wt_wr + i * 4096) = W3[i];
      __syncthreads();
#pragma unroll
      for (int ks = 0; ks < 12; ++ks) {
        const v2u t0 = tr_read(smem + wt_rd + ks * 4096), t1 = tr_read(smem + wt_rd + ks * 4096 + 512);
        bf[ks] = v4u{t0.x, t0.y, t1.x, t1.y};
      }
      SB();
      bool ok = true;
#pragma unroll
      for (int k = 0; k < 24; ++k) ok &= (unsigned)(zv[k] >> 32) == tag;
      if (__builtin_expect(!__all(ok), 0)) {
        unsigned spins = 0;
        do {
#pragma unroll
          for (int k = 0; k < 6; ++k)
#pragma unroll
            for (int e = 0; e < 4; ++e) zv[4 * k + e] = ld_gran(src + zo[k] + e * NCOL);
          ok = true;
#pragma unroll
          for (int k = 0; k < 24; ++k) ok &= (unsigned)(zv[k] >> 32) == tag;
        } while (!__all(ok) && ++spins < SPIN_CAP);
      }
#pragma unroll
      for (int k = 0; k < 6; ++k) {
        const int pi = tid + 256 * k, bb = pi / 384, row = pi - bb * 384;
        const float z = fmaxf((__uint_as_float((unsigned)zv[4 * k]) + __uint_as_float((unsigned)zv[4 * k + 1])) + (__uint_as_float((unsigned)zv[4 * k + 2]) + __uint_as_float((unsigned)zv[4 * k + 3])), 0.f);
        *reinterpret_cast<u16*>(smem + L_Z + bb * 832 + row * 2) = (u16)f2bf(z);
      }
      __syncthreads();
#pragma unroll
      for (int ks = 0; ks < 12; ++ks) af[ks] = *reinterpret_cast<const v4u*>(smem + L_Z + q4 * 832 + (32 * ks + 8 * g4) * 2);
      SB();
      f32x4 d = {0.f, 0.f, 0.f, 0.f}, d2 = {0.f, 0.f, 0.f, 0.f};
#pragma unroll
      for (int ks = 0; ks < 12; ks += 2) { d = mfma16(af[ks], bf[ks], d); d2 = mfma16(af[ks + 1], bf[ks + 1], d2); }
      __builtin_amdgcn_raw_buffer_store_b64(v2u{__float_as_uint(d[0] + d2[0]), tag}, rsW, pgo, 0, AUX_SC1);
    }
  } else if (wg < 240) {
    const int item = (wg - 200) * 4 + w, fcb = item >> 4, fb = (item >> 2) & 3, fq = item & 3;
    float gs = 0.f;
#pragma unroll
    for (int k = 0; k < 10; ++k) gs = fmaf(bf2f(p.x[fb * DM + 64 * k + lane]), bf2f(p.gw[64 * k + lane]), gs);
    gs = wave_sum(gs);
    float g = 1.f / (1.f + __expf(-gs));
    asm volatile("" : "+v"(g));
    const unsigned po = (unsigned)(WS_PG + (size_t)((g4 * 10 + fcb) * 256 + fb * 64 + fq * 16 + (lane & 15)) * 8);
    v2u S[5];
    {
      unsigned spins = 0;
      bool ok;
      do {
        asm volatile("" ::: "memory");
#pragma unroll
        for (int i = 0; i < 5; ++i) S[i] = __builtin_amdgcn_raw_buffer_load_b64(rsW, po, i * (4 * 10 * 256 * 8), AUX_SC1);
        ok = __all((((S[0].y ^ tag) | (S[1].y ^ tag) | (S[2].y ^ tag)) | ((S[3].y ^ tag) | (S[4].y ^ tag))) == 0u);
      } while (!ok && ++spins < SPIN_CAP);
    }
    float res = g * ((__uint_as_float(S[0].x) + __uint_as_float(S[1].x)) + (__uint_as_float(S[2].x) + __uint_as_float(S[3].x))) + (1.f - g) * __uint_as_float(S[4].x);
    {
      auto t16 = __builtin_amdgcn_permlane16_swap(__float_as_uint(res), __float_as_uint(res), false, false);
      res = __uint_as_float(t16[0]) + __uint_as_float(t16[1]);
      auto t32 = __builtin_amdgcn_permlane32_swap(__float_as_uint(res), __float_as_uint(res), false, false);
      res = __uint_as_float(t32[0]) + __uint_as_float(t32[1]);
    }
    if (lane < 16) p.out[fb * DM + fcb * 64 + fq * 16 + lane] = (u16)f2bf(res);
    if (item == 0 && lane == 0) __hip_atomic_store((gu32*)(p.ws + WS_EPOCH), ep + 1u, RLX_AGENT);
    if (item == 0) { const unsigned now = (unsigned)__builtin_amdgcn_s_memrealtime(); const unsigned tgt = now + 4 * (now - td0); while ((int)((unsigned)__builtin_amdgcn_s_memrealtime() - tgt) < 0) __builtin_amdgcn_s_sleep(4); }
  }
}

extern "C" void kernel_launch(void* const* d_in, const int* in_sizes, int n_in, void* d_out, int out_size, void* d_ws, size_t ws_size, hipStream_t stream) {
  Params p;
  p.x = (const u16*)d_in[0]; p.kc = (const u16*)d_in[1]; p.vc = (const u16*)d_in[2]; p.wq = (const u16*)d_in[3]; p.wo = (const u16*)d_in[4];
  p.w1 = (const u16*)d_in[6]; p.w2 = (const u16*)d_in[7]; p.gw = (const u16*)d_in[8]; p.out = (u16*)d_out; p.ws = (unsigned char*)d_ws;
  hipLaunchKernelGGL(gated_mix_decode_kernel, dim3(NWG), dim3(NTHR), 0, stream, p);
}
```

```cpp
#include <hip/hip_runtime.h>
#include <hip/hip_bf16.h>
#include <stdint.h>

typedef unsigned short u16;
typedef unsigned long long u64;
typedef unsigned v4u __attribute__((ext_vector_type(4)));
typedef unsigned v2u __attribute__((ext_vector_type(2)));
typedef float f32x4 __attribute__((ext_vector_type(4)));
typedef float f32x2 __attribute__((ext_vector_type(2)));
typedef __bf16 bf16x2 __attribute__((ext_vector_type(2)));
typedef __bf16 bf16x8 __attribute__((ext_vector_type(8)));
typedef short v4i16_t __attribute__((ext_vector_type(4)));
#define GAS __attribute__((address_space(1)))
#define LAS __attribute__((address_space(3)))
typedef GAS unsigned gu32;
typedef GAS unsigned long long gu64;
#define RLX_AGENT __ATOMIC_RELAXED, __HIP_MEMORY_SCOPE_AGENT

constexpr int NB = 4, DM = 640, NH = 32, SK = 4096, DH = 192, WIN = 1024, DFF = 1536, NQ = NH * DH, NCOL = NQ + DFF;
constexpr int NWG = 256, NTHR = 256;
constexpr float SCL2 = 0.07216878364870322f * 1.4426950408889634f;
constexpr int WINDOW = 16;
constexpr int QPOS = 23;
constexpr int MLPOS = 32;
constexpr int ZPOS = 8;
constexpr int PACE = 240;
constexpr int PACE_HEAD = -70, PACE_FROM = WINDOW + 10, PACE_EVERY = 3, PACE_PHASE = 2, TB_RD = 6;
constexpr int AUX_NT = 2, AUX_SC1 = 16;
constexpr unsigned CODE_PF_OFF = 11264, CODE_PF_LEN = 5504;
constexpr size_t WS_EPOCH = 0;
constexpr size_t WS_T0 = 256;
constexpr size_t WS_S1 = 4096;
constexpr size_t WS_ML = WS_S1 + (size_t)NB * 2 * NCOL * 8;
constexpr size_t WS_OG = WS_ML + (size_t)NB * NH * 16 * 8;
constexpr size_t WS_PG = WS_OG + (size_t)NB * 2 * (NQ / 2) * 8;
constexpr size_t WS_END = WS_PG + (size_t)20 * 10 * NB * 64 * 8;
constexpr unsigned SPIN_CAP = 1u << 16;
constexpr unsigned OOB = 0xFFFFFFF0u;
constexpr int L_WT = 0;
constexpr int L_Z = L_WT + 384 * 128;
constexpr int L_P = L_Z + 4 * 832;
constexpr int L_Q = L_P + 4 * 128 * 4;
constexpr int L_O = L_Q + 4 * 384;
constexpr int L_USED = L_O + 4 * 192 * 4;
constexpr int L_BYTES = 84 * 1024;
static_assert(L_USED <= L_BYTES, "lds");

struct Params {
  const u16* x; const u16* kc; const u16* vc; const u16* wq; const u16* wo; const u16* w1; const u16* w2; const u16* gw;
  u16* out; unsigned char* ws;
};

__device__ __forceinline__ float f_lo(unsigned v) { return __uint_as_float(v << 16); }
__device__ __forceinline__ float f_hi(unsigned v) { return __uint_as_float(v & 0xffff0000u); }
__device__ __forceinline__ float bf2f(u16 v) { return __uint_as_float((unsigned)v << 16); }
__device__ __forceinline__ float dot2(unsigned k, unsigned q, float acc) {
  return __builtin_amdgcn_fdot2_f32_bf16(__builtin_bit_cast(bf16x2, k), __builtin_bit_cast(bf16x2, q), acc, false);
}
template <int CTRL> __device__ __forceinline__ float dpp(float x) {
  return __builtin_bit_cast(float, __builtin_amdgcn_mov_dpp(__builtin_bit_cast(int, x), CTRL, 0xf, 0xf, true));
}
constexpr int XOR1 = 0xB1, XOR2 = 0x4E, XOR7 = 0x141, XOR8 = 0x128;
__device__ __forceinline__ float swap32_add(float a, float b) {
  auto t = __builtin_amdgcn_permlane32_swap(__float_as_uint(a), __float_as_uint(b), false, false);
  return __uint_as_float(t[0]) + __uint_as_float(t[1]);
}
__device__ __forceinline__ float swap16_add(float a, float b) {
  auto t = __builtin_amdgcn_permlane16_swap(__float_as_uint(a), __float_as_uint(b), false, false);
  return __uint_as_float(t[0]) + __uint_as_float(t[1]);
}
__device__ __forceinline__ float wave_max(float x) {
  x = fmaxf(x, dpp<XOR1>(x)); x = fmaxf(x, dpp<XOR2>(x)); x = fmaxf(x, dpp<XOR7>(x)); x = fmaxf(x, dpp<XOR8>(x));
  auto s = __builtin_amdgcn_permlane16_swap(__float_as_uint(x), __float_as_uint(x), false, false);
  x = fmaxf(__uint_as_float(s[0]), __uint_as_float(s[1]));
  auto t = __builtin_amdgcn_permlane32_swap(__float_as_uint(x), __float_as_uint(x), false, false);
  return fmaxf(__uint_as_float(t[0]), __uint_as_float(t[1]));
}
__device__ __forceinline__ float wave_sum(float x) {
  x += dpp<XOR1>(x); x += dpp<XOR2>(x); x += dpp<XOR7>(x); x += dpp<XOR8>(x);
  auto s = __builtin_amdgcn_permlane16_swap(__float_as_uint(x), __float_as_uint(x), false, false);
  x = __uint_as_float(s[0]) + __uint_as_float(s[1]);
  auto t = __builtin_amdgcn_permlane32_swap(__float_as_uint(x), __float_as_uint(x), false, false);
  return __uint_as_float(t[0]) + __uint_as_float(t[1]);
}
__device__ __forceinline__ unsigned f2bf(float f) { return (unsigned)__builtin_bit_cast(u16, (__bf16)f); }
__device__ __forceinline__ unsigned pk2bf(float lo, float hi) { bf16x2 v = {(__bf16)lo, (__bf16)hi}; return __builtin_bit_cast(unsigned, v); }
__device__ __forceinline__ u64 ld_gran(gu64* g) { return __hip_atomic_load(g, RLX_AGENT); }
__device__ __forceinline__ __amdgpu_buffer_rsrc_t mk_rsrc(const void* p, unsigned bytes) {
  return __builtin_amdgcn_make_buffer_rsrc(const_cast<void*>(p), 0, bytes, 0x00020000);
}
__device__ __forceinline__ f32x4 mfma16(v4u a, v4u b, f32x4 c) {
  return __builtin_amdgcn_mfma_f32_16x16x32_bf16(__builtin_bit_cast(bf16x8, a), __builtin_bit_cast(bf16x8, b), c, 0, 0, 0);
}
__device__ __forceinline__ v2u tr_read(const unsigned char* p) {
  return __builtin_bit_cast(v2u, __builtin_amdgcn_ds_read_tr16_b64_v4i16((LAS v4i16_t*)p));
}
#define SB() __builtin_amdgcn_sched_barrier(0)
#define WAITFOR(v) do { asm volatile("" ::"v"(v)); SB(); } while (0)

__global__ void __launch_bounds__(NTHR, 1) gated_mix_decode_kernel(Params p) {
  __shared__ __attribute__((aligned(16))) unsigned char smem[L_BYTES];
  const int tid = threadIdx.x, lane = tid & 63, w = __builtin_amdgcn_readfirstlane(tid >> 6), r = lane >> 3, c = lane & 7;
  const int wg = blockIdx.x, bh = (wg & 7) * 16 + (wg >> 4), s = (wg >> 3) & 1, b = bh >> 5, h = bh & 31;
  constexpr int W = WINDOW;
  unsigned tzero = (unsigned)__builtin_amdgcn_s_memrealtime();
  u64 pc0;
  asm volatile("s_getpc_b64 %0" : "=s"(pc0));
  unsigned zoff = 0;
  asm volatile("" : "+v"(zoff));
  const unsigned ep = __hip_atomic_load((gu32*)(p.ws + WS_EPOCH + zoff), RLX_AGENT);
  const bool has1 = wg >= 16;
  const int u1 = has1 ? wg - 16 : 0, lc = u1 >> 1, half = u1 & 1;
  const bool isq = lc < 96;
  const __amdgpu_buffer_rsrc_t rs1 = mk_rsrc(isq ? p.wq : p.w1, has1 ? (isq ? DM * NQ * 2 : DM * DFF * 2) : 0);
  const unsigned ld1 = isq ? NQ * 2 : DFF * 2;
  const unsigned vo1 = (320 * half + 8 * w + r) * ld1 + (isq ? lc : lc - 96) * 128 + 16 * c;
  const size_t kvoff = ((size_t)bh * SK + (SK - WIN) + 512 * s) * DH;
  const __amdgpu_buffer_rsrc_t rsK = mk_rsrc(p.kc + kvoff, 512 * DH * 2), rsV = mk_rsrc(p.vc + kvoff, 512 * DH * 2);
  const unsigned vkv = w * 3072 + r * 384 + c * 16;
  const bool has3 = wg < 200;
  const int rb = wg / 10, cb = wg - rb * 10;
  const bool iso = rb < 16;
  const __amdgpu_buffer_rsrc_t rs3 = mk_rsrc(iso ? p.wo : p.w2, has3 ? (iso ? NQ * DM * 2 : DFF * DM * 2) : 0);
  const unsigned vo3 = ((iso ? rb : rb - 16) * 384 + 8 * w + r) * (DM * 2) + cb * 128 + 16 * c;
  const __amdgpu_buffer_rsrc_t rsW = mk_rsrc(p.ws, (unsigned)WS_END);
  const __amdgpu_buffer_rsrc_t rsX = mk_rsrc(p.x, NB * DM * 2);
  v4u W1[10], K[48], V[48], W3[12], XA[10];
#define ISSUE(n_) do { const int n__ = (n_); \
    if (n__ >= PACE_FROM && n__ < 118 && ((n__ + PACE_PHASE) % PACE_EVERY) == 0) { \
      const unsigned tgt_ = tzero + (unsigned)(PACE_HEAD + ((n__ * PACE) >> 4)); \
      while ((int)((unsigned)__builtin_amdgcn_s_memrealtime() - tgt_) < 0) __builtin_amdgcn_s_sleep(1); \
      SB(); } \
    if (n__ < 10) W1[n__] = __builtin_amdgcn_raw_buffer_load_b128(rs1, vo1, n__ * 32 * ld1, AUX_NT); \
    else if (n__ < 58) K[n__ - 10] = __builtin_amdgcn_raw_buffer_load_b128(rsK, vkv + ((n__ - 10) % 3) * 128, ((n__ - 10) / 3) * 12288, AUX_NT); \
    else if (n__ < 106) V[n__ - 58] = __builtin_amdgcn_raw_buffer_load_b128(rsV, vkv + ((n__ - 58) % 3) * 128, ((n__ - 58) / 3) * 12288, AUX_NT); \
    else if (n__ < 118) W3[n__ - 106] = __builtin_amdgcn_raw_buffer_load_b128(rs3, vo3, (n__ - 106) * 32 * (DM * 2), AUX_NT); \
    SB(); } while (0)
  const unsigned wt_wr = L_WT + (8 * w + r) * 128 + ((((c >> 1) ^ (((r >> 1) & 1) | ((w & 1) << 1))) << 5) | ((c & 1) << 4));
  const int g4 = lane >> 4, q4 = (lane & 15) >> 2, p4 = lane & 3;
  const unsigned wt_rd = L_WT + (8 * g4 + q4) * 128 + (((w ^ (((q4 >> 1) & 1) | ((g4 & 1) << 1))) << 5) | (p4 << 3));

#pragma unroll
  for (int n = 0; n < 10; ++n) ISSUE(n);
  {
    const unsigned xo = (q4 * DM + 320 * half + 8 * g4) * 2;
#pragma unroll
    for (int i = 0; i < 10; ++i) { XA[i] = __builtin_amdgcn_raw_buffer_load_b128(rsX, xo + 64 * i, 0, 0); SB(); }
  }
#pragma unroll
  for (int n = 10; n < W; ++n) ISSUE(n);
#pragma unroll
  for (int i = 0; i < 10; ++i) {
    *reinterpret_cast<v4u*>(smem + wt_wr + i * 4096) = W1[i];
    SB();
    ISSUE(W + i);
  }
  const unsigned tag = ep * 0x9E3779B1u + 0x7F4A7C15u;
  __syncthreads();
  {
    f32x4 d = {0.f, 0.f, 0.f, 0.f};
#pragma unroll
    for (int ks = 0; ks < 10; ++ks) {
      const v2u t0 = tr_read(smem + wt_rd + ks * 4096), t1 = tr_read(smem + wt_rd + ks * 4096 + 512);
      d = mfma16(XA[ks], v4u{t0.x, t0.y, t1.x, t1.y}, d);
    }
    const unsigned off = has1 ? (unsigned)(WS_S1 + (size_t)((g4 * 2 + half) * NCOL + 64 * lc + 16 * w + (lane & 15)) * 8) : OOB;
    __builtin_amdgcn_raw_buffer_store_b64(v2u{__float_as_uint(d[0]), tag}, rsW, off, 0, AUX_SC1);
    __builtin_amdgcn_raw_buffer_store_b64(v2u{tzero, tag}, rsW, (wg == 0 && tid == 0) ? (unsigned)WS_T0 : OOB, 0, AUX_SC1);
    SB();
  }
  u64 t0g = 0;
#pragma unroll
  for (int n = W + 10; n <= 10 + QPOS; ++n) {
    WAITFOR(K[n - W - 10]); ISSUE(n);
    if (n == W + 10 + TB_RD) { t0g = ld_gran((gu64*)(p.ws + WS_T0)); SB(); }
  }
  gu64* qg = (gu64*)(p.ws + WS_S1) + (size_t)b * 2 * NCOL + h * DH + lane;
  u64 qv[6];
#pragma unroll
  for (int j = 0; j < 3; ++j) { qv[2 * j] = ld_gran(qg + 64 * j); qv[2 * j + 1] = ld_gran(qg + NCOL + 64 * j); }
  SB();
#pragma unroll
  for (int n = 10 + QPOS + 1; n <= 10 + QPOS + W; ++n) { WAITFOR(K[n - W - 10]); ISSUE(n); }
  {
    bool ok = true;
#pragma unroll
    for (int j = 0; j < 6; ++j) ok &= (unsigned)(qv[j] >> 32) == tag;
    if (__builtin_expect(!__all(ok), 0)) {
      unsigned spins = 0;
      do {
        __builtin_amdgcn_s_sleep(2);
#pragma unroll
        for (int j = 0; j < 3; ++j) { qv[2 * j] = ld_gran(qg + 64 * j); qv[2 * j + 1] = ld_gran(qg + NCOL + 64 * j); }
        ok = true;
#pragma unroll
        for (int j = 0; j < 6; ++j) ok &= (unsigned)(qv[j] >> 32) == tag;
      } while (!__all(ok) && ++spins < SPIN_CAP);
    }
  }
  {
    const unsigned tv = __builtin_amdgcn_readfirstlane((unsigned)t0g), tg = __builtin_amdgcn_readfirstlane((unsigned)(t0g >> 32));
    const int d = (int)(tzero - tv);
    if (tg == (unsigned)__builtin_amdgcn_readfirstlane(tag) && d > 0 && d < 300) tzero = tv;
  }
  v4u qp[3];
  {
    u16* ql = reinterpret_cast<u16*>(smem + L_Q + w * 384);
#pragma unroll
    for (int j = 0; j < 3; ++j) ql[64 * j + lane] = (u16)f2bf(__uint_as_float((unsigned)qv[2 * j]) + __uint_as_float((unsigned)qv[2 * j + 1]));
    asm volatile("s_waitcnt lgkmcnt(0)" ::: "memory");
#pragma unroll
    for (int j = 0; j < 3; ++j) qp[j] = *reinterpret_cast<const v4u*>(smem + L_Q + w * 384 + j * 128 + c * 16);
  }
  float sc[16];
#define QK_GROUP(i) do { float d_ = 0.f; \
    _Pragma("unroll") for (int j = 0; j < 3; ++j) _Pragma("unroll") for (int k = 0; k < 4; ++k) d_ = dot2(K[3 * (i) + j][k], qp[j][k], d_); \
    sc[i] = d_; asm volatile("" : "+v"(sc[i])); SB(); } while (0)
  constexpr int G0 = (QPOS + 1) / 3;
#pragma unroll
  for (int i = 0; i < G0; ++i) QK_GROUP(i);
#pragma unroll
  for (int m = 3 * G0; m < 48; ++m) {
    if (m % 3 == 2) { QK_GROUP(m / 3); } else if (m > QPOS) { WAITFOR(K[m]); }
    if (m > QPOS) ISSUE(10 + m + W);
  }
#define TR_STEP(BASE, HALF, CTRL, BIT) _Pragma("unroll") for (int q_ = 0; q_ < (HALF); ++q_) { \
    const float t1_ = sc[BASE + q_] + dpp<CTRL>(sc[BASE + q_]), t2_ = sc[BASE + q_ + (HALF)] + dpp<CTRL>(sc[BASE + q_ + (HALF)]); sc[BASE + q_] = (c & (BIT)) ? t2_ : t1_; }
  TR_STEP(0, 4, XOR7, 4) TR_STEP(0, 2, XOR2, 2) TR_STEP(0, 1, XOR1, 1)
  TR_STEP(8, 4, XOR7, 4) TR_STEP(8, 2, XOR2, 2) TR_STEP(8, 1, XOR1, 1)
#undef TR_STEP
  const float sa = sc[0] * SCL2, sb = sc[8] * SCL2;
  const float mw = wave_max(fmaxf(sa, sb));
  const float pa = __builtin_amdgcn_exp2f(sa - mw), pb = __builtin_amdgcn_exp2f(sb - mw);
  const float lw = wave_sum(pa + pb);
  {
    const unsigned off = lane < 2 ? (unsigned)(WS_ML + (size_t)(bh * 16 + (s * 4 + w) * 2 + lane) * 8) : OOB;
    __builtin_amdgcn_raw_buffer_store_b64(v2u{__float_as_uint(lane == 0 ? mw : lw), tag}, rsW, off, 0, AUX_SC1);
  }
  *reinterpret_cast<float*>(smem + L_P + (w * 128 + lane) * 4) = pa;
  *reinterpret_cast<float*>(smem + L_P + (w * 128 + 64 + lane) * 4) = pb;
  asm volatile("s_waitcnt lgkmcnt(0)" ::: "memory");
  f32x4 pv[4];
  pv[0] = *reinterpret_cast<const f32x4*>(smem + L_P + (w * 128 + 8 * r) * 4);
  pv[1] = *reinterpret_cast<const f32x4*>(smem + L_P + (w * 128 + 8 * r + 4) * 4);
  pv[2] = *reinterpret_cast<const f32x4*>(smem + L_P + (w * 128 + 64 + 8 * r) * 4);
  pv[3] = *reinterpret_cast<const f32x4*>(smem + L_P + (w * 128 + 64 + 8 * r + 4) * 4);
  gu64* mlg = (gu64*)(p.ws + WS_ML) + (size_t)bh * 16 + (lane & 15);
  u64 mlv = 0;
  unsigned cpf = 0;
  float fac = 0.f;
  f32x2 acc[3][4];
#pragma unroll
  for (int j = 0; j < 3; ++j)
#pragma unroll
    for (int k = 0; k < 4; ++k) acc[j][k] = f32x2{0.f, 0.f};
#pragma unroll
  for (int m = 0; m < 48; ++m) {
    const int i = m / 3, j = m % 3;
    const float pi = pv[i >> 2][i & 3];
    const f32x2 pp = {pi, pi};
#pragma unroll
    for (int k = 0; k < 4; ++k) acc[j][k] = __builtin_elementwise_fma(pp, f32x2{f_lo(V[m][k]), f_hi(V[m][k])}, acc[j][k]);
    asm volatile("" : "+v"(acc[j][0]), "+v"(acc[j][1]), "+v"(acc[j][2]), "+v"(acc[j][3]));
    SB();
    ISSUE(58 + m + W);
    if (m + W == MLPOS) {
      mlv = ld_gran(mlg);
      cpf = __builtin_amdgcn_raw_buffer_load_b32(mk_rsrc((const void*)(pc0 + CODE_PF_OFF), w == 0 ? CODE_PF_LEN : 0), lane * 128, 0, 0);
      SB();
    }
    if (m == MLPOS + 1) {
      if (__builtin_expect(!__all((unsigned)(mlv >> 32) == tag), 0)) {
        unsigned spins = 0;
        do {
          __builtin_amdgcn_s_sleep(1);
          mlv = ld_gran(mlg);
        } while (!__all((unsigned)(mlv >> 32) == tag) && ++spins < SPIN_CAP);
      }
      const float val = __uint_as_float((unsigned)mlv);
      const bool odd = lane & 1;
      const float M = wave_max(odd ? -INFINITY : val);
      const float mp = dpp<XOR1>(val);
      const float t = (odd && lane < 16) ? __builtin_amdgcn_exp2f(mp - M) * val : 0.f;
      const float L = wave_sum(t);
      fac = __builtin_amdgcn_exp2f(mw - M) / L;
      asm volatile("" : "+v"(fac));
      asm volatile("" ::"v"(cpf));
      SB();
    }
    if (m >= 45) {
      float a4[4], a2[2];
#pragma unroll
      for (int e = 0; e < 4; ++e) a4[e] = swap32_add(acc[j][e >> 1][e & 1], acc[j][(e + 4) >> 1][e & 1]);
#pragma unroll
      for (int e = 0; e < 2; ++e) a2[e] = swap16_add(a4[e], a4[e + 2]);
      const float t1 = a2[0] + dpp<XOR8>(a2[0]), t2 = a2[1] + dpp<XOR8>(a2[1]);
      *reinterpret_cast<float*>(smem + L_O + (w * 192 + 64 * j + 8 * c + r) * 4) = ((lane & 8) ? t2 : t1) * fac;
      SB();
    }
  }
#pragma unroll
  for (int n = 58 + 48 + W; n < 118; ++n) ISSUE(n);
  __syncthreads();
  {
    const int t = tid < 96 ? tid : 95;
    f32x2 a = {0.f, 0.f};
#pragma unroll
    for (int ww = 0; ww < 4; ++ww) a += *reinterpret_cast<const f32x2*>(smem + L_O + (ww * 192 + 2 * t) * 4);
    const unsigned off = tid < 96 ? (unsigned)(WS_OG + (size_t)((b * 2 + s) * (NQ / 2) + h * (DH / 2) + tid) * 8) : OOB;
    __builtin_amdgcn_raw_buffer_store_b64(v2u{pk2bf(a.x, a.y), tag}, rsW, off, 0, AUX_SC1);
    SB();
  }
  if (__builtin_expect(has3, 1)) {
    v4u bf[12], af[12];
    const unsigned pgo = (unsigned)(WS_PG + (size_t)((rb * 10 + cb) * 256 + g4 * 64 + 16 * w + (lane & 15)) * 8);
    if (__builtin_expect(iso, 1)) {
      const int i1 = tid < 128 ? tid + 256 : tid, b0 = tid / 96, k0 = tid - b0 * 96, b1 = i1 / 96, k1 = i1 - b1 * 96;
      const unsigned zo0 = (unsigned)(WS_OG + (size_t)(b0 * 2 * (NQ / 2) + rb * 192 + 2 * k0) * 8);
      const unsigned zo1 = (unsigned)(WS_OG + (size_t)(b1 * 2 * (NQ / 2) + rb * 192 + 2 * k1) * 8);
      const unsigned zl0 = L_Z + b0 * 832 + k0 * 8, zl1 = L_Z + b1 * 832 + k1 * 8;
      v4u A[4], B[4];
#define ZISSUE(S) do { asm volatile("" ::: "memory");   \
        S[0] = __builtin_amdgcn_raw_buffer_load_b128(rsW, zo0, 0, AUX_SC1); S[1] = __builtin_amdgcn_raw_buffer_load_b128(rsW, zo0, (NQ / 2) * 8, AUX_SC1); \
        S[2] = __builtin_amdgcn_raw_buffer_load_b128(rsW, zo1, 0, AUX_SC1); S[3] = __builtin_amdgcn_raw_buffer_load_b128(rsW, zo1, (NQ / 2) * 8, AUX_SC1); SB(); } while (0)
#define ZOK(S) __all((((S[0].y ^ tag) | (S[0].w ^ tag) | (S[1].y ^ tag)) | ((S[1].w ^ tag) | (S[2].y ^ tag) | (S[2].w ^ tag)) | ((S[3].y ^ tag) | (S[3].w ^ tag))) == 0u)
#define ZPUT(S) do { \
        v2u z0 = {pk2bf(f_lo(S[0].x) + f_lo(S[1].x), f_hi(S[0].x) + f_hi(S[1].x)), pk2bf(f_lo(S[0].z) + f_lo(S[1].z), f_hi(S[0].z) + f_hi(S[1].z))}; \
        *reinterpret_cast<v2u*>(smem + zl0) = z0; \
        v2u z1 = {pk2bf(f_lo(S[2].x) + f_lo(S[3].x), f_hi(S[2].x) + f_hi(S[3].x)), pk2bf(f_lo(S[2].z) + f_lo(S[3].z), f_hi(S[2].z) + f_hi(S[3].z))}; \
        *reinterpret_cast<v2u*>(smem + zl1) = z1; SB(); } while (0)
#pragma unroll
      for (int i = 0; i < ZPOS; ++i) { *reinterpret_cast<v4u*>(smem + wt_wr + i * 4096) = W3[i]; SB(); }
      ZISSUE(A);
#pragma unroll
      for (int i = ZPOS; i < 12; ++i) { *reinterpret_cast<v4u*>(smem + wt_wr + i * 4096) = W3[i]; SB(); }
      ZISSUE(B);
      __syncthreads();
#pragma unroll
      for (int ks = 0; ks < 12; ++ks) {
        const v2u t0 = tr_read(smem + wt_rd + ks * 4096), t1 = tr_read(smem + wt_rd + ks * 4096 + 512);
        bf[ks] = v4u{t0.x, t0.y, t1.x, t1.y};
      }
      SB();
      if (__builtin_expect(ZOK(A), 1)) { ZPUT(A); }
      else if (__builtin_expect(ZOK(B), 1)) { ZPUT(B); }
      else {
        v4u C[4];
        unsigned spins = 0;
        do { ZISSUE(C); } while (!ZOK(C) && ++spins < SPIN_CAP);
        ZPUT(C);
      }
      __syncthreads();
#pragma unroll
      for (int ks = 0; ks < 12; ++ks) af[ks] = *reinterpret_cast<const v4u*>(smem + L_Z + q4 * 832 + (32 * ks + 8 * g4) * 2);
      SB();
      f32x4 d = {0.f, 0.f, 0.f, 0.f}, d2 = {0.f, 0.f, 0.f, 0.f};
#pragma unroll
      for (int ks = 0; ks < 12; ks += 2) { d = mfma16(af[ks], bf[ks], d); d2 = mfma16(af[ks + 1], bf[ks + 1], d2); }
      __builtin_amdgcn_raw_buffer_store_b64(v2u{__float_as_uint(d[0] + d2[0]), tag}, rsW, pgo, 0, AUX_SC1);
      SB();
#pragma unroll
      for (int k = 0; k < 4; ++k) { asm volatile("" ::"v"(A[k])); asm volatile("" ::"v"(B[k])); }
    } else {
      gu64* src = (gu64*)(p.ws + WS_S1) + NQ + (rb - 16) * 384;
      u64 zv[12];
      int zo[6];
#pragma unroll
      for (int k = 0; k < 6; ++k) { const int pi = tid + 256 * k, bb = pi / 384, row = pi - bb * 384; zo[k] = bb * 2 * NCOL + row; }
#pragma unroll
      for (int k = 0; k < 6; ++k) { zv[2 * k] = ld_gran(src + zo[k]); zv[2 * k + 1] = ld_gran(src + zo[k] + NCOL); }
      SB();
#pragma unroll
      for (int i = 0; i < 12; ++i) *reinterpret_cast<v4u*>(smem + wt_wr + i * 4096) = W3[i];
      __syncthreads();
#pragma unroll
      for (int ks = 0; ks < 12; ++ks) {
        const v2u t0 = tr_read(smem + wt_rd + ks * 4096), t1 = tr_read(smem + wt_rd + ks * 4096 + 512);
        bf[ks] = v4u{t0.x, t0.y, t1.x, t1.y};
      }
      SB();
      bool ok = true;
#pragma unroll
      for (int k = 0; k < 12; ++k) ok &= (unsigned)(zv[k] >> 32) == tag;
      if (__builtin_expect(!__all(ok), 0)) {
        unsigned spins = 0;
        do {
#pragma unroll
          for (int k = 0; k < 6; ++k) { zv[2 * k] = ld_gran(src + zo[k]); zv[2 * k + 1] = ld_gran(src + zo[k] + NCOL); }
          ok = true;
#pragma unroll
          for (int k = 0; k < 12; ++k) ok &= (unsigned)(zv[k] >> 32) == tag;
        } while (!__all(ok) && ++spins < SPIN_CAP);
      }
#pragma unroll
      for (int k = 0; k < 6; ++k) {
        const int pi = tid + 256 * k, bb = pi / 384, row = pi - bb * 384;
        const float z = fmaxf(__uint_as_float((unsigned)zv[2 * k]) + __uint_as_float((unsigned)zv[2 * k + 1]), 0.f);
        *reinterpret_cast<u16*>(smem + L_Z + bb * 832 + row * 2) = (u16)f2bf(z);
      }
      __syncthreads();
#pragma unroll
      for (int ks = 0; ks < 12; ++ks) af[ks] = *reinterpret_cast<const v4u*>(smem + L_Z + q4 * 832 + (32 * ks + 8 * g4) * 2);
      SB();
      f32x4 d = {0.f, 0.f, 0.f, 0.f}, d2 = {0.f, 0.f, 0.f, 0.f};
#pragma unroll
      for (int ks = 0; ks < 12; ks += 2) { d = mfma16(af[ks], bf[ks], d); d2 = mfma16(af[ks + 1], bf[ks + 1], d2); }
      __builtin_amdgcn_raw_buffer_store_b64(v2u{__float_as_uint(d[0] + d2[0]), tag}, rsW, pgo, 0, AUX_SC1);
    }
  } else if (wg < 240) {
    const int item = (wg - 200) * 4 + w, fcb = item >> 4, fb = (item >> 2) & 3, fq = item & 3;
    float gs = 0.f;
#pragma unroll
    for (int k = 0; k < 10; ++k) gs = fmaf(bf2f(p.x[fb * DM + 64 * k + lane]), bf2f(p.gw[64 * k + lane]), gs);
    gs = wave_sum(gs);
    float g = 1.f / (1.f + __expf(-gs));
    asm volatile("" : "+v"(g));
    const unsigned po = (unsigned)(WS_PG + (size_t)((g4 * 10 + fcb) * 256 + fb * 64 + fq * 16 + (lane & 15)) * 8);
    v2u S[5];
    {
      unsigned spins = 0;
      bool ok;
      do {
        asm volatile("" ::: "memory");
#pragma unroll
        for (int i = 0; i < 5; ++i) S[i] = __builtin_amdgcn_raw_buffer_load_b64(rsW, po, i * (4 * 10 * 256 * 8), AUX_SC1);
        ok = __all((((S[0].y ^ tag) | (S[1].y ^ tag) | (S[2].y ^ tag)) | ((S[3].y ^ tag) | (S[4].y ^ tag))) == 0u);
      } while (!ok && ++spins < SPIN_CAP);
    }
    float res = g * ((__uint_as_float(S[0].x) + __uint_as_float(S[1].x)) + (__uint_as_float(S[2].x) + __uint_as_float(S[3].x))) + (1.f - g) * __uint_as_float(S[4].x);
    {
      auto t16 = __builtin_amdgcn_permlane16_swap(__float_as_uint(res), __float_as_uint(res), false, false);
      res = __uint_as_float(t16[0]) + __uint_as_float(t16[1]);
      auto t32 = __builtin_amdgcn_permlane32_swap(__float_as_uint(res), __float_as_uint(res), false, false);
      res = __uint_as_float(t32[0]) + __uint_as_float(t32[1]);
    }
    if (lane < 16) p.out[fb * DM + fcb * 64 + fq * 16 + lane] = (u16)f2bf(res);
    if (item == 0 && lane == 0) __hip_atomic_store((gu32*)(p.ws + WS_EPOCH), ep + 1u, RLX_AGENT);
  }
}

extern "C" void kernel_launch(void* const* d_in, const int* in_sizes, int n_in, void* d_out, int out_size, void* d_ws, size_t ws_size, hipStream_t stream) {
  Params p;
  p.x = (const u16*)d_in[0]; p.kc = (const u16*)d_in[1]; p.vc = (const u16*)d_in[2]; p.wq = (const u16*)d_in[3]; p.wo = (const u16*)d_in[4];
  p.w1 = (const u16*)d_in[6]; p.w2 = (const u16*)d_in[7]; p.gw = (const u16*)d_in[8]; p.out = (u16*)d_out; p.ws = (unsigned char*)d_ws;
  hipLaunchKernelGGL(gated_mix_decode_kernel, dim3(NWG), dim3(NTHR), 0, stream, p);
}
```

```cpp
#include <hip/hip_runtime.h>
#include <hip/hip_bf16.h>
#include <stdint.h>

typedef unsigned short u16;
typedef unsigned long long u64;
typedef unsigned v4u __attribute__((ext_vector_type(4)));
typedef unsigned v2u __attribute__((ext_vector_type(2)));
typedef float f32x4 __attribute__((ext_vector_type(4)));
typedef float f32x2 __attribute__((ext_vector_type(2)));
typedef __bf16 bf16x2 __attribute__((ext_vector_type(2)));
typedef __bf16 bf16x8 __attribute__((ext_vector_type(8)));
typedef short v4i16_t __attribute__((ext_vector_type(4)));
#define GAS __attribute__((address_space(1)))
#define LAS __attribute__((address_space(3)))
typedef GAS unsigned gu32;
typedef GAS unsigned long long gu64;
#define RLX_AGENT __ATOMIC_RELAXED, __HIP_MEMORY_SCOPE_AGENT

constexpr int NB = 4, DM = 640, NH = 32, SK = 4096, DH = 192, WIN = 1024, DFF = 1536, NQ = NH * DH, NCOL = NQ + DFF;
constexpr int NWG = 256, NTHR = 256;
constexpr float SCL2 = 0.07216878364870322f * 1.4426950408889634f;
constexpr int WINDOW = 16;
constexpr int QPOS = 23;
constexpr int MLPOS = 32;
constexpr int ZPOS = 8;
constexpr int PACE = 236;
constexpr int PACE_HEAD = -70, PACE_FROM = WINDOW + 10, PACE_EVERY = 3, PACE_PHASE = 2, TB_RD = 6;
constexpr int AUX_NT = 2, AUX_SC1 = 16;
constexpr unsigned CODE_PF_OFF = 11264, CODE_PF_LEN = 5248;
constexpr size_t WS_EPOCH = 0;
constexpr size_t WS_T0 = 256;
constexpr size_t WS_S1 = 4096;
constexpr size_t WS_ML = WS_S1 + (size_t)NB * 2 * NCOL * 8;
constexpr size_t WS_OG = WS_ML + (size_t)NB * NH * 16 * 8;
constexpr size_t WS_PG = WS_OG + (size_t)NB * 2 * (NQ / 2) * 8;
constexpr size_t WS_END = WS_PG + (size_t)20 * 10 * NB * 64 * 8;
constexpr unsigned SPIN_CAP = 1u << 16;
constexpr unsigned OOB = 0xFFFFFFF0u;
constexpr int L_WT = 0;
constexpr int L_Z = L_WT + 384 * 128;
constexpr int L_P = L_Z + 4 * 832;
constexpr int L_Q = L_P + 4 * 128 * 4;
constexpr int L_O = L_Q + 4 * 384;
constexpr int L_USED = L_O + 4 * 192 * 4;
constexpr int L_BYTES = 84 * 1024;
static_assert(L_USED <= L_BYTES, "lds");

struct Params {
  const u16* x; const u16* kc; const u16* vc; const u16* wq; const u16* wo; const u16* w1; const u16* w2; const u16* gw;
  u16* out; unsigned char* ws;
};

__device__ __forceinline__ float f_lo(unsigned v) { return __uint_as_float(v << 16); }
__device__ __forceinline__ float f_hi(unsigned v) { return __uint_as_float(v & 0xffff0000u); }
__device__ __forceinline__ float bf2f(u16 v) { return __uint_as_float((unsigned)v << 16); }
__device__ __forceinline__ float dot2(unsigned k, unsigned q, float acc) {
  return __builtin_amdgcn_fdot2_f32_bf16(__builtin_bit_cast(bf16x2, k), __builtin_bit_cast(bf16x2, q), acc, false);
}
template <int CTRL> __device__ __forceinline__ float dpp(float x) {
  return __builtin_bit_cast(float, __builtin_amdgcn_mov_dpp(__builtin_bit_cast(int, x), CTRL, 0xf, 0xf, true));
}
constexpr int XOR1 = 0xB1, XOR2 = 0x4E, XOR7 = 0x141, XOR8 = 0x128;
__device__ __forceinline__ float swap32_add(float a, float b) {
  auto t = __builtin_amdgcn_permlane32_swap(__float_as_uint(a), __float_as_uint(b), false, false);
  return __uint_as_float(t[0]) + __uint_as_float(t[1]);
}
__device__ __forceinline__ float swap16_add(float a, float b) {
  auto t = __builtin_amdgcn_permlane16_swap(__float_as_uint(a), __float_as_uint(b), false, false);
  return __uint_as_float(t[0]) + __uint_as_float(t[1]);
}
__device__ __forceinline__ float wave_max(float x) {
  x = fmaxf(x, dpp<XOR1>(x)); x = fmaxf(x, dpp<XOR2>(x)); x = fmaxf(x, dpp<XOR7>(x)); x = fmaxf(x, dpp<XOR8>(x));
  auto s = __builtin_amdgcn_permlane16_swap(__float_as_uint(x), __float_as_uint(x), false, false);
  x = fmaxf(__uint_as_float(s[0]), __uint_as_float(s[1]));
  auto t = __builtin_amdgcn_permlane32_swap(__float_as_uint(x), __float_as_uint(x), false, false);
  return fmaxf(__uint_as_float(t[0]), __uint_as_float(t[1]));
}
__device__ __forceinline__ float wave_sum(float x) {
  x += dpp<XOR1>(x); x += dpp<XOR2>(x); x += dpp<XOR7>(x); x += dpp<XOR8>(x);
  auto s = __builtin_amdgcn_permlane16_swap(__float_as_uint(x), __float_as_uint(x), false, false);
  x = __uint_as_float(s[0]) + __uint_as_float(s[1]);
  auto t = __builtin_amdgcn_permlane32_swap(__float_as_uint(x), __float_as_uint(x), false, false);
  return __uint_as_float(t[0]) + __uint_as_float(t[1]);
}
__device__ __forceinline__ unsigned f2bf(float f) { return (unsigned)__builtin_bit_cast(u16, (__bf16)f); }
__device__ __forceinline__ unsigned pk2bf(float lo, float hi) { bf16x2 v = {(__bf16)lo, (__bf16)hi}; return __builtin_bit_cast(unsigned, v); }
__device__ __forceinline__ u64 ld_gran(gu64* g) { return __hip_atomic_load(g, RLX_AGENT); }
__device__ __forceinline__ __amdgpu_buffer_rsrc_t mk_rsrc(const void* p, unsigned bytes) {
  return __builtin_amdgcn_make_buffer_rsrc(const_cast<void*>(p), 0, bytes, 0x00020000);
}
__device__ __forceinline__ f32x4 mfma16(v4u a, v4u b, f32x4 c) {
  return __builtin_amdgcn_mfma_f32_16x16x32_bf16(__builtin_bit_cast(bf16x8, a), __builtin_bit_cast(bf16x8, b), c, 0, 0, 0);
}
__device__ __forceinline__ v2u tr_read(const unsigned char* p) {
  return __builtin_bit_cast(v2u, __builtin_amdgcn_ds_read_tr16_b64_v4i16((LAS v4i16_t*)p));
}
#define SB() __builtin_amdgcn_sched_barrier(0)
#define WAITFOR(v) do { asm volatile("" ::"v"(v)); SB(); } while (0)

__global__ void __launch_bounds__(NTHR, 1) gated_mix_decode_kernel(Params p) {
  __shared__ __attribute__((aligned(16))) unsigned char smem[L_BYTES];
  const int tid = threadIdx.x, lane = tid & 63, w = __builtin_amdgcn_readfirstlane(tid >> 6), r = lane >> 3, c = lane & 7;
  const int wg = blockIdx.x, bh = (wg & 7) * 16 + (wg >> 4), s = (wg >> 3) & 1, b = bh >> 5, h = bh & 31;
  constexpr int W = WINDOW;
  unsigned tzero = (unsigned)__builtin_amdgcn_s_memrealtime();
  const unsigned td0 = tzero;
  u64 pc0;
  asm volatile("s_getpc_b64 %0" : "=s"(pc0));
  unsigned zoff = 0;
  asm volatile("" : "+v"(zoff));
  const unsigned ep = __hip_atomic_load((gu32*)(p.ws + WS_EPOCH + zoff), RLX_AGENT);
  const bool has1 = wg >= 16;
  const int u1 = has1 ? wg - 16 : 0, lc = u1 >> 1, half = u1 & 1;
  const bool isq = lc < 96;
  const __amdgpu_buffer_rsrc_t rs1 = mk_rsrc(isq ? p.wq : p.w1, has1 ? (isq ? DM * NQ * 2 : DM * DFF * 2) : 0);
  const unsigned ld1 = isq ? NQ * 2 : DFF * 2;
  const unsigned vo1 = (320 * half + 8 * w + r) * ld1 + (isq ? lc : lc - 96) * 128 + 16 * c;
  const size_t kvoff = ((size_t)bh * SK + (SK - WIN) + 512 * s) * DH;
  const __amdgpu_buffer_rsrc_t rsK = mk_rsrc(p.kc + kvoff, 512 * DH * 2), rsV = mk_rsrc(p.vc + kvoff, 512 * DH * 2);
  const unsigned vkv = w * 3072 + r * 384 + c * 16;
  const bool has3 = wg < 200;
  const int rb = wg / 10, cb = wg - rb * 10;
  const bool iso = rb < 16;
  const __amdgpu_buffer_rsrc_t rs3 = mk_rsrc(iso ? p.wo : p.w2, has3 ? (iso ? NQ * DM * 2 : DFF * DM * 2) : 0);
  const unsigned vo3 = ((iso ? rb : rb - 16) * 384 + 8 * w + r) * (DM * 2) + cb * 128 + 16 * c;
  const __amdgpu_buffer_rsrc_t rsW = mk_rsrc(p.ws, (unsigned)WS_END);
  const __amdgpu_buffer_rsrc_t rsX = mk_rsrc(p.x, NB * DM * 2);
  v4u W1[10], K[48], V[48], W3[12], XA[10];
#define ISSUE(n_) do { const int n__ = (n_); \
    if (n__ >= PACE_FROM && n__ < 118 && ((n__ + PACE_PHASE) % PACE_EVERY) == 0) { \
      const unsigned tgt_ = tzero + (unsigned)(PACE_HEAD + ((n__ * PACE) >> 4)); \
      while ((int)((unsigned)__builtin_amdgcn_s_memrealtime() - tgt_) < 0) __builtin_amdgcn_s_sleep(1); \
      SB(); } \
    if (n__ < 10) W1[n__] = __builtin_amdgcn_raw_buffer_load_b128(rs1, vo1, n__ * 32 * ld1, AUX_NT); \
    else if (n__ < 58) K[n__ - 10] = __builtin_amdgcn_raw_buffer_load_b128(rsK, vkv + ((n__ - 10) % 3) * 128, ((n__ - 10) / 3) * 12288, AUX_NT); \
    else if (n__ < 106) V[n__ - 58] = __builtin_amdgcn_raw_buffer_load_b128(rsV, vkv + ((n__ - 58) % 3) * 128, ((n__ - 58) / 3) * 12288, AUX_NT); \
    else if (n__ < 118) W3[n__ - 106] = __builtin_amdgcn_raw_buffer_load_b128(rs3, vo3, (n__ - 106) * 32 * (DM * 2), AUX_NT); \
    SB(); } while (0)
  const unsigned wt_wr = L_WT + (8 * w + r) * 128 + ((((c >> 1) ^ (((r >> 1) & 1) | ((w & 1) << 1))) << 5) | ((c & 1) << 4));
  const int g4 = lane >> 4, q4 = (lane & 15) >> 2, p4 = lane & 3;
  const unsigned wt_rd = L_WT + (8 * g4 + q4) * 128 + (((w ^ (((q4 >> 1) & 1) | ((g4 & 1) << 1))) << 5) | (p4 << 3));

#pragma unroll
  for (int n = 0; n < 10; ++n) ISSUE(n);
  {
    const unsigned xo = (q4 * DM + 320 * half + 8 * g4) * 2;
#pragma unroll
    for (int i = 0; i < 10; ++i) { XA[i] = __builtin_amdgcn_raw_buffer_load_b128(rsX, xo + 64 * i, 0, 0); SB(); }
  }
#pragma unroll
  for (int n = 10; n < W; ++n) ISSUE(n);
  const bool hasf = wg >= 200 && wg < 240;
  v4u GX0, GX1, GW0, GW1;
  {
    const __amdgpu_buffer_rsrc_t rsGx = mk_rsrc(p.x + (wg & 3) * DM, hasf ? DM * 2 : 0), rsGw = mk_rsrc(p.gw, hasf ? DM * 2 : 0);
    GX0 = __builtin_amdgcn_raw_buffer_load_b128(rsGx, lane * 16, 0, 0); GX1 = __builtin_amdgcn_raw_buffer_load_b128(rsGx, lane * 16, 1024, 0);
    GW0 = __builtin_amdgcn_raw_buffer_load_b128(rsGw, lane * 16, 0, 0); GW1 = __builtin_amdgcn_raw_buffer_load_b128(rsGw, lane * 16, 1024, 0);
    SB();
  }
#pragma unroll
  for (int i = 0; i < 10; ++i) {
    *reinterpret_cast<v4u*>(smem + wt_wr + i * 4096) = W1[i];
    SB();
    ISSUE(W + i);
  }
  const unsigned tag = ep * 0x9E3779B1u + 0x7F4A7C15u;
  __syncthreads();
  {
    f32x4 d = {0.f, 0.f, 0.f, 0.f};
#pragma unroll
    for (int ks = 0; ks < 10; ++ks) {
      const v2u t0 = tr_read(smem + wt_rd + ks * 4096), t1 = tr_read(smem + wt_rd + ks * 4096 + 512);
      d = mfma16(XA[ks], v4u{t0.x, t0.y, t1.x, t1.y}, d);
    }
    const unsigned off = has1 ? (unsigned)(WS_S1 + (size_t)((g4 * 2 + half) * NCOL + 64 * lc + 16 * w + (lane & 15)) * 8) : OOB;
    __builtin_amdgcn_raw_buffer_store_b64(v2u{__float_as_uint(d[0]), tag}, rsW, off, 0, AUX_SC1);
    __builtin_amdgcn_raw_buffer_store_b64(v2u{tzero, tag}, rsW, (wg == 0 && tid == 0) ? (unsigned)WS_T0 : OOB, 0, AUX_SC1);
    SB();
  }
  float gate;
  {
    float gs = 0.f;
#pragma unroll
    for (int k = 0; k < 4; ++k) { gs = dot2(GX0[k], GW0[k], gs); gs = dot2(GX1[k], GW1[k], gs); }
    gs = wave_sum(gs);
    gate = 1.f / (1.f + __expf(-gs));
    asm volatile("" : "+v"(gate));
    SB();
  }
  u64 t0g = 0;
#pragma unroll
  for (int n = W + 10; n <= 10 + QPOS; ++n) {
    WAITFOR(K[n - W - 10]); ISSUE(n);
    if (n == W + 10 + TB_RD) { t0g = ld_gran((gu64*)(p.ws + WS_T0)); SB(); }
  }
  gu64* qg = (gu64*)(p.ws + WS_S1) + (size_t)b * 2 * NCOL + h * DH + lane;
  u64 qv[6];
#pragma unroll
  for (int j = 0; j < 3; ++j) { qv[2 * j] = ld_gran(qg + 64 * j); qv[2 * j + 1] = ld_gran(qg + NCOL + 64 * j); }
  SB();
#pragma unroll
  for (int n = 10 + QPOS + 1; n <= 10 + QPOS + W; ++n) { WAITFOR(K[n - W - 10]); ISSUE(n); }
  {
    bool ok = true;
#pragma unroll
    for (int j = 0; j < 6; ++j) ok &= (unsigned)(qv[j] >> 32) == tag;
    if (__builtin_expect(!__all(ok), 0)) {
      unsigned spins = 0;
      do {
        __builtin_amdgcn_s_sleep(2);
#pragma unroll
        for (int j = 0; j < 3; ++j) { qv[2 * j] = ld_gran(qg + 64 * j); qv[2 * j + 1] = ld_gran(qg + NCOL + 64 * j); }
        ok = true;
#pragma unroll
        for (int j = 0; j < 6; ++j) ok &= (unsigned)(qv[j] >> 32) == tag;
      } while (!__all(ok) && ++spins < SPIN_CAP);
    }
  }
  {
    const unsigned tv = __builtin_amdgcn_readfirstlane((unsigned)t0g), tg = __builtin_amdgcn_readfirstlane((unsigned)(t0g >> 32));
    const int d = (int)(tzero - tv);
    if (tg == (unsigned)__builtin_amdgcn_readfirstlane(tag) && d > 0 && d < 300) tzero = tv;
  }
  v4u qp[3];
  {
    u16* ql = reinterpret_cast<u16*>(smem + L_Q + w * 384);
#pragma unroll
    for (int j = 0; j < 3; ++j) ql[64 * j + lane] = (u16)f2bf(__uint_as_float((unsigned)qv[2 * j]) + __uint_as_float((unsigned)qv[2 * j + 1]));
    asm volatile("s_waitcnt lgkmcnt(0)" ::: "memory");
#pragma unroll
    for (int j = 0; j < 3; ++j) qp[j] = *reinterpret_cast<const v4u*>(smem + L_Q + w * 384 + j * 128 + c * 16);
  }
  float sc[16];
#define QK_GROUP(i) do { float d_ = 0.f; \
    _Pragma("unroll") for (int j = 0; j < 3; ++j) _Pragma("unroll") for (int k = 0; k < 4; ++k) d_ = dot2(K[3 * (i) + j][k], qp[j][k], d_); \
    sc[i] = d_; asm volatile("" : "+v"(sc[i])); SB(); } while (0)
  constexpr int G0 = (QPOS + 1) / 3;
#pragma unroll
  for (int i = 0; i < G0; ++i) QK_GROUP(i);
#pragma unroll
  for (int m = 3 * G0; m < 48; ++m) {
    if (m % 3 == 2) { QK_GROUP(m / 3); } else if (m > QPOS) { WAITFOR(K[m]); }
    if (m > QPOS) ISSUE(10 + m + W);
  }
#define TR_STEP(BASE, HALF, CTRL, BIT) _Pragma("unroll") for (int q_ = 0; q_ < (HALF); ++q_) { \
    const float t1_ = sc[BASE + q_] + dpp<CTRL>(sc[BASE + q_]), t2_ = sc[BASE + q_ + (HALF)] + dpp<CTRL>(sc[BASE + q_ + (HALF)]); sc[BASE + q_] = (c & (BIT)) ? t2_ : t1_; }
  TR_STEP(0, 4, XOR7, 4) TR_STEP(0, 2, XOR2, 2) TR_STEP(0, 1, XOR1, 1)
  TR_STEP(8, 4, XOR7, 4) TR_STEP(8, 2, XOR2, 2) TR_STEP(8, 1, XOR1, 1)
#undef TR_STEP
  const float sa = sc[0] * SCL2, sb = sc[8] * SCL2;
  const float mw = wave_max(fmaxf(sa, sb));
  const float pa = __builtin_amdgcn_exp2f(sa - mw), pb = __builtin_amdgcn_exp2f(sb - mw);
  const float lw = wave_sum(pa + pb);
  {
    const unsigned off = lane < 2 ? (unsigned)(WS_ML + (size_t)(bh * 16 + (s * 4 + w) * 2 + lane) * 8) : OOB;
    __builtin_amdgcn_raw_buffer_store_b64(v2u{__float_as_uint(lane == 0 ? mw : lw), tag}, rsW, off, 0, AUX_SC1);
  }
  *reinterpret_cast<float*>(smem + L_P + (w * 128 + lane) * 4) = pa;
  *reinterpret_cast<float*>(smem + L_P + (w * 128 + 64 + lane) * 4) = pb;
  asm volatile("s_waitcnt lgkmcnt(0)" ::: "memory");
  f32x4 pv[4];
  pv[0] = *reinterpret_cast<const f32x4*>(smem + L_P + (w * 128 + 8 * r) * 4);
  pv[1] = *reinterpret_cast<const f32x4*>(smem + L_P + (w * 128 + 8 * r + 4) * 4);
  pv[2] = *reinterpret_cast<const f32x4*>(smem + L_P + (w * 128 + 64 + 8 * r) * 4);
  pv[3] = *reinterpret_cast<const f32x4*>(smem + L_P + (w * 128 + 64 + 8 * r + 4) * 4);
  gu64* mlg = (gu64*)(p.ws + WS_ML) + (size_t)bh * 16 + (lane & 15);
  u64 mlv = 0;
  unsigned cpf = 0;
  float fac = 0.f;
  f32x2 acc[3][4];
#pragma unroll
  for (int j = 0; j < 3; ++j)
#pragma unroll
    for (int k = 0; k < 4; ++k) acc[j][k] = f32x2{0.f, 0.f};
#pragma unroll
  for (int m = 0; m < 48; ++m) {
    const int i = m / 3, j = m % 3;
    const float pi = pv[i >> 2][i & 3];
    const f32x2 pp = {pi, pi};
#pragma unroll
    for (int k = 0; k < 4; ++k) acc[j][k] = __builtin_elementwise_fma(pp, f32x2{f_lo(V[m][k]), f_hi(V[m][k])}, acc[j][k]);
    asm volatile("" : "+v"(acc[j][0]), "+v"(acc[j][1]), "+v"(acc[j][2]), "+v"(acc[j][3]));
    SB();
    ISSUE(58 + m + W);
    if (m + W == MLPOS) {
      mlv = ld_gran(mlg);
      cpf = __builtin_amdgcn_raw_buffer_load_b32(mk_rsrc((const void*)(pc0 + CODE_PF_OFF), w == 0 ? CODE_PF_LEN : 0), lane * 128, 0, 0);
      SB();
    }
    if (m == MLPOS + 1) {
      if (__builtin_expect(!__all((unsigned)(mlv >> 32) == tag), 0)) {
        unsigned spins = 0;
        do {
          __builtin_amdgcn_s_sleep(1);
          mlv = ld_gran(mlg);
        } while (!__all((unsigned)(mlv >> 32) == tag) && ++spins < SPIN_CAP);
      }
      const float val = __uint_as_float((unsigned)mlv);
      const bool odd = lane & 1;
      const float M = wave_max(odd ? -INFINITY : val);
      const float mp = dpp<XOR1>(val);
      const float t = (odd && lane < 16) ? __builtin_amdgcn_exp2f(mp - M) * val : 0.f;
      const float L = wave_sum(t);
      fac = __builtin_amdgcn_exp2f(mw - M) / L;
      asm volatile("" : "+v"(fac));
      asm volatile("" ::"v"(cpf));
      SB();
    }
    if (m >= 45) {
      float a4[4], a2[2];
#pragma unroll
      for (int e = 0; e < 4; ++e) a4[e] = swap32_add(acc[j][e >> 1][e & 1], acc[j][(e + 4) >> 1][e & 1]);
#pragma unroll
      for (int e = 0; e < 2; ++e) a2[e] = swap16_add(a4[e], a4[e + 2]);
      const float t1 = a2[0] + dpp<XOR8>(a2[0]), t2 = a2[1] + dpp<XOR8>(a2[1]);
      *reinterpret_cast<float*>(smem + L_O + (w * 192 + 64 * j + 8 * c + r) * 4) = ((lane & 8) ? t2 : t1) * fac;
      SB();
    }
  }
#pragma unroll
  for (int n = 58 + 48 + W; n < 118; ++n) ISSUE(n);
  __syncthreads();
  {
    const int t = tid < 96 ? tid : 95;
    f32x2 a = {0.f, 0.f};
#pragma unroll
    for (int ww = 0; ww < 4; ++ww) a += *reinterpret_cast<const f32x2*>(smem + L_O + (ww * 192 + 2 * t) * 4);
    const unsigned off = tid < 96 ? (unsigned)(WS_OG + (size_t)((b * 2 + s) * (NQ / 2) + h * (DH / 2) + tid) * 8) : OOB;
    __builtin_amdgcn_raw_buffer_store_b64(v2u{pk2bf(a.x, a.y), tag}, rsW, off, 0, AUX_SC1);
    SB();
  }
  if (__builtin_expect(has3, 1)) {
    v4u bf[12], af[12];
    const unsigned pgo = (unsigned)(WS_PG + (size_t)((rb * 10 + cb) * 256 + g4 * 64 + 16 * w + (lane & 15)) * 8);
    if (__builtin_expect(iso, 1)) {
      const int i1 = tid < 128 ? tid + 256 : tid, b0 = tid / 96, k0 = tid - b0 * 96, b1 = i1 / 96, k1 = i1 - b1 * 96;
      const unsigned zo0 = (unsigned)(WS_OG + (size_t)(b0 * 2 * (NQ / 2) + rb * 192 + 2 * k0) * 8);
      const unsigned zo1 = (unsigned)(WS_OG + (size_t)(b1 * 2 * (NQ / 2) + rb * 192 + 2 * k1) * 8);
      const unsigned zl0 = L_Z + b0 * 832 + k0 * 8, zl1 = L_Z + b1 * 832 + k1 * 8;
      v4u A[4], B[4];
#define ZISSUE(S) do { asm volatile("" ::: "memory");   \
        S[0] = __builtin_amdgcn_raw_buffer_load_b128(rsW, zo0, 0, AUX_SC1); S[1] = __builtin_amdgcn_raw_buffer_load_b128(rsW, zo0, (NQ / 2) * 8, AUX_SC1); \
        S[2] = __builtin_amdgcn_raw_buffer_load_b128(rsW, zo1, 0, AUX_SC1); S[3] = __builtin_amdgcn_raw_buffer_load_b128(rsW, zo1, (NQ / 2) * 8, AUX_SC1); SB(); } while (0)
#define ZOK(S) __all((((S[0].y ^ tag) | (S[0].w ^ tag) | (S[1].y ^ tag)) | ((S[1].w ^ tag) | (S[2].y ^ tag) | (S[2].w ^ tag)) | ((S[3].y ^ tag) | (S[3].w ^ tag))) == 0u)
#define ZPUT(S) do { \
        v2u z0 = {pk2bf(f_lo(S[0].x) + f_lo(S[1].x), f_hi(S[0].x) + f_hi(S[1].x)), pk2bf(f_lo(S[0].z) + f_lo(S[1].z), f_hi(S[0].z) + f_hi(S[1].z))}; \
        *reinterpret_cast<v2u*>(smem + zl0) = z0; \
        v2u z1 = {pk2bf(f_lo(S[2].x) + f_lo(S[3].x), f_hi(S[2].x) + f_hi(S[3].x)), pk2bf(f_lo(S[2].z) + f_lo(S[3].z), f_hi(S[2].z) + f_hi(S[3].z))}; \
        *reinterpret_cast<v2u*>(smem + zl1) = z1; SB(); } while (0)
#pragma unroll
      for (int i = 0; i < ZPOS; ++i) { *reinterpret_cast<v4u*>(smem + wt_wr + i * 4096) = W3[i]; SB(); }
      ZISSUE(A);
#pragma unroll
      for (int i = ZPOS; i < 12; ++i) { *reinterpret_cast<v4u*>(smem + wt_wr + i * 4096) = W3[i]; SB(); }
      ZISSUE(B);
      __syncthreads();
#pragma unroll
      for (int ks = 0; ks < 12; ++ks) {
        const v2u t0 = tr_read(smem + wt_rd + ks * 4096), t1 = tr_read(smem + wt_rd + ks * 4096 + 512);
        bf[ks] = v4u{t0.x, t0.y, t1.x, t1.y};
      }
      SB();
      if (__builtin_expect(ZOK(A), 1)) { ZPUT(A); }
      else if (__builtin_expect(ZOK(B), 1)) { ZPUT(B); }
      else {
        v4u C[4];
        unsigned spins = 0;
        do { ZISSUE(C); } while (!ZOK(C) && ++spins < SPIN_CAP);
        ZPUT(C);
      }
      __syncthreads();
#pragma unroll
      for (int ks = 0; ks < 12; ++ks) af[ks] = *reinterpret_cast<const v4u*>(smem + L_Z + q4 * 832 + (32 * ks + 8 * g4) * 2);
      SB();
      f32x4 d = {0.f, 0.f, 0.f, 0.f}, d2 = {0.f, 0.f, 0.f, 0.f};
#pragma unroll
      for (int ks = 0; ks < 12; ks += 2) { d = mfma16(af[ks], bf[ks], d); d2 = mfma16(af[ks + 1], bf[ks + 1], d2); }
      __builtin_amdgcn_raw_buffer_store_b64(v2u{__float_as_uint(d[0] + d2[0]), tag}, rsW, pgo, 0, AUX_SC1);
      SB();
#pragma unroll
      for (int k = 0; k < 4; ++k) { asm volatile("" ::"v"(A[k])); asm volatile("" ::"v"(B[k])); }
    } else {
      gu64* src = (gu64*)(p.ws + WS_S1) + NQ + (rb - 16) * 384;
      u64 zv[12];
      int zo[6];
#pragma unroll
      for (int k = 0; k < 6; ++k) { const int pi = tid + 256 * k, bb = pi / 384, row = pi - bb * 384; zo[k] = bb * 2 * NCOL + row; }
#pragma unroll
      for (int k = 0; k < 6; ++k) { zv[2 * k] = ld_gran(src + zo[k]); zv[2 * k + 1] = ld_gran(src + zo[k] + NCOL); }
      SB();
#pragma unroll
      for (int i = 0; i < 12; ++i) *reinterpret_cast<v4u*>(smem + wt_wr + i * 4096) = W3[i];
      __syncthreads();
#pragma unroll
      for (int ks = 0; ks < 12; ++ks) {
        const v2u t0 = tr_read(smem + wt_rd + ks * 4096), t1 = tr_read(smem + wt_rd + ks * 4096 + 512);
        bf[ks] = v4u{t0.x, t0.y, t1.x, t1.y};
      }
      SB();
      bool ok = true;
#pragma unroll
      for (int k = 0; k < 12; ++k) ok &= (unsigned)(zv[k] >> 32) == tag;
      if (__builtin_expect(!__all(ok), 0)) {
        unsigned spins = 0;
        do {
#pragma unroll
          for (int k = 0; k < 6; ++k) { zv[2 * k] = ld_gran(src + zo[k]); zv[2 * k + 1] = ld_gran(src + zo[k] + NCOL); }
          ok = true;
#pragma unroll
          for (int k = 0; k < 12; ++k) ok &= (unsigned)(zv[k] >> 32) == tag;
        } while (!__all(ok) && ++spins < SPIN_CAP);
      }
#pragma unroll
      for (int k = 0; k < 6; ++k) {
        const int pi = tid + 256 * k, bb = pi / 384, row = pi - bb * 384;
        const float z = fmaxf(__uint_as_float((unsigned)zv[2 * k]) + __uint_as_float((unsigned)zv[2 * k + 1]), 0.f);
        *reinterpret_cast<u16*>(smem + L_Z + bb * 832 + row * 2) = (u16)f2bf(z);
      }
      __syncthreads();
#pragma unroll
      for (int ks = 0; ks < 12; ++ks) af[ks] = *reinterpret_cast<const v4u*>(smem + L_Z + q4 * 832 + (32 * ks + 8 * g4) * 2);
      SB();
      f32x4 d = {0.f, 0.f, 0.f, 0.f}, d2 = {0.f, 0.f, 0.f, 0.f};
#pragma unroll
      for (int ks = 0; ks < 12; ks += 2) { d = mfma16(af[ks], bf[ks], d); d2 = mfma16(af[ks + 1], bf[ks + 1], d2); }
      __builtin_amdgcn_raw_buffer_store_b64(v2u{__float_as_uint(d[0] + d2[0]), tag}, rsW, pgo, 0, AUX_SC1);
    }
  } else if (hasf) {
    const int item = (wg - 200) * 4 + w, fcb = item >> 4, fb = (item >> 2) & 3, fq = item & 3;
    const float g = gate;
    const unsigned po = (unsigned)(WS_PG + (size_t)((g4 * 10 + fcb) * 256 + fb * 64 + fq * 16 + (lane & 15)) * 8);
    v2u S[5];
    {
      unsigned spins = 0;
      bool ok;
      do {
        asm volatile("" ::: "memory");
#pragma unroll
        for (int i = 0; i < 5; ++i) S[i] = __builtin_amdgcn_raw_buffer_load_b64(rsW, po, i * (4 * 10 * 256 * 8), AUX_SC1);
        ok = __all((((S[0].y ^ tag) | (S[1].y ^ tag) | (S[2].y ^ tag)) | ((S[3].y ^ tag) | (S[4].y ^ tag))) == 0u);
      } while (!ok && ++spins < SPIN_CAP);
    }
    float res = g * ((__uint_as_float(S[0].x) + __uint_as_float(S[1].x)) + (__uint_as_float(S[2].x) + __uint_as_float(S[3].x))) + (1.f - g) * __uint_as_float(S[4].x);
    {
      auto t16 = __builtin_amdgcn_permlane16_swap(__float_as_uint(res), __float_as_uint(res), false, false);
      res = __uint_as_float(t16[0]) + __uint_as_float(t16[1]);
      auto t32 = __builtin_amdgcn_permlane32_swap(__float_as_uint(res), __float_as_uint(res), false, false);
      res = __uint_as_float(t32[0]) + __uint_as_float(t32[1]);
    }
    if (lane < 16) p.out[fb * DM + fcb * 64 + fq * 16 + lane] = (u16)f2bf(res);
    if (item == 0 && lane == 0) __hip_atomic_store((gu32*)(p.ws + WS_EPOCH), ep + 1u, RLX_AGENT);
    if (item == 0) { const unsigned now = (unsigned)__builtin_amdgcn_s_memrealtime(); const unsigned tgt = now + 4 * (now - td0); while ((int)((unsigned)__builtin_amdgcn_s_memrealtime() - tgt) < 0) __builtin_amdgcn_s_sleep(4); }
  }
}

extern "C" void kernel_launch(void* const* d_in, const int* in_sizes, int n_in, void* d_out, int out_size, void* d_ws, size_t ws_size, hipStream_t stream) {
  Params p;
  p.x = (const u16*)d_in[0]; p.kc = (const u16*)d_in[1]; p.vc = (const u16*)d_in[2]; p.wq = (const u16*)d_in[3]; p.wo = (const u16*)d_in[4];
  p.w1 = (const u16*)d_in[6]; p.w2 = (const u16*)d_in[7]; p.gw = (const u16*)d_in[8]; p.out = (u16*)d_out; p.ws = (unsigned char*)d_ws;
  hipLaunchKernelGGL(gated_mix_decode_kernel, dim3(NWG), dim3(NTHR), 0, stream, p);
}
```

```cpp
#include <hip/hip_runtime.h>
#include <hip/hip_bf16.h>
#include <stdint.h>

typedef unsigned short u16;
typedef unsigned long long u64;
typedef unsigned v4u __attribute__((ext_vector_type(4)));
typedef unsigned v2u __attribute__((ext_vector_type(2)));
typedef float f32x4 __attribute__((ext_vector_type(4)));
typedef float f32x2 __attribute__((ext_vector_type(2)));
typedef __bf16 bf16x2 __attribute__((ext_vector_type(2)));
typedef __bf16 bf16x8 __attribute__((ext_vector_type(8)));
typedef short v4i16_t __attribute__((ext_vector_type(4)));
#define GAS __attribute__((address_space(1)))
#define LAS __attribute__((address_space(3)))
typedef GAS unsigned gu32;
typedef GAS unsigned long long gu64;
#define RLX_AGENT __ATOMIC_RELAXED, __HIP_MEMORY_SCOPE_AGENT

constexpr int NB = 4, DM = 640, NH = 32, SK = 4096, DH = 192, WIN = 1024, DFF = 1536, NQ = NH * DH, NCOL = NQ + DFF;
constexpr int NWG = 256, NTHR = 256;
constexpr float SCL2 = 0.07216878364870322f * 1.4426950408889634f;
constexpr int WINDOW = 16;
constexpr int QPOS = 23;
constexpr int MLPOS = 32;
constexpr int ZPOS = 8;
constexpr int PACE = 236;
constexpr int PACE_HEAD = -70, PACE_FROM = WINDOW + 10, PACE_EVERY = 3, PACE_PHASE = 2, TB_RD = 6;
constexpr int AUX_NT = 2, AUX_SC1 = 16;
constexpr unsigned CODE_PF_OFF = 11264, CODE_PF_LEN = 5504;
constexpr size_t WS_EPOCH = 0;
constexpr size_t WS_T0 = 256;
constexpr size_t WS_S1 = 4096;
constexpr size_t WS_ML = WS_S1 + (size_t)NB * 2 * NCOL * 8;
constexpr size_t WS_OG = WS_ML + (size_t)NB * NH * 16 * 8;
constexpr size_t WS_PG = WS_OG + (size_t)NB * 2 * (NQ / 2) * 8;
constexpr size_t WS_END = WS_PG + (size_t)20 * 10 * NB * 64 * 8;
constexpr unsigned SPIN_CAP = 1u << 16;
constexpr unsigned OOB = 0xFFFFFFF0u;
constexpr int L_WT = 0;
constexpr int L_Z = L_WT + 384 * 128;
constexpr int L_P = L_Z + 4 * 832;
constexpr int L_Q = L_P + 4 * 128 * 4;
constexpr int L_O = L_Q + 4 * 384;
constexpr int L_USED = L_O + 4 * 192 * 4;
constexpr int L_BYTES = 84 * 1024;
static_assert(L_USED <= L_BYTES, "lds");

struct Params {
  const u16* x; const u16* kc; const u16* vc; const u16* wq; const u16* wo; const u16* w1; const u16* w2; const u16* gw;
  u16* out; unsigned char* ws;
};

__device__ __forceinline__ float f_lo(unsigned v) { return __uint_as_float(v << 16); }
__device__ __forceinline__ float f_hi(unsigned v) { return __uint_as_float(v & 0xffff0000u); }
__device__ __forceinline__ float bf2f(u16 v) { return __uint_as_float((unsigned)v << 16); }
__device__ __forceinline__ float dot2(unsigned k, unsigned q, float acc) {
  return __builtin_amdgcn_fdot2_f32_bf16(__builtin_bit_cast(bf16x2, k), __builtin_bit_cast(bf16x2, q), acc, false);
}
template <int CTRL> __device__ __forceinline__ float dpp(float x) {
  return __builtin_bit_cast(float, __builtin_amdgcn_mov_dpp(__builtin_bit_cast(int, x), CTRL, 0xf, 0xf, true));
}
constexpr int XOR1 = 0xB1, XOR2 = 0x4E, XOR7 = 0x141, XOR8 = 0x128;
__device__ __forceinline__ float swap32_add(float a, float b) {
  auto t = __builtin_amdgcn_permlane32_swap(__float_as_uint(a), __float_as_uint(b), false, false);
  return __uint_as_float(t[0]) + __uint_as_float(t[1]);
}
__device__ __forceinline__ float swap16_add(float a, float b) {
  auto t = __builtin_amdgcn_permlane16_swap(__float_as_uint(a), __float_as_uint(b), false, false);
  return __uint_as_float(t[0]) + __uint_as_float(t[1]);
}
__device__ __forceinline__ float wave_max(float x) {
  x = fmaxf(x, dpp<XOR1>(x)); x = fmaxf(x, dpp<XOR2>(x)); x = fmaxf(x, dpp<XOR7>(x)); x = fmaxf(x, dpp<XOR8>(x));
  auto s = __builtin_amdgcn_permlane16_swap(__float_as_uint(x), __float_as_uint(x), false, false);
  x = fmaxf(__uint_as_float(s[0]), __uint_as_float(s[1]));
  auto t = __builtin_amdgcn_permlane32_swap(__float_as_uint(x), __float_as_uint(x), false, false);
  return fmaxf(__uint_as_float(t[0]), __uint_as_float(t[1]));
}
__device__ __forceinline__ float wave_sum(float x) {
  x += dpp<XOR1>(x); x += dpp<XOR2>(x); x += dpp<XOR7>(x); x += dpp<XOR8>(x);
  auto s = __builtin_amdgcn_permlane16_swap(__float_as_uint(x), __float_as_uint(x), false, false);
  x = __uint_as_float(s[0]) + __uint_as_float(s[1]);
  auto t = __builtin_amdgcn_permlane32_swap(__float_as_uint(x), __float_as_uint(x), false, false);
  return __uint_as_float(t[0]) + __uint_as_float(t[1]);
}
__device__ __forceinline__ unsigned f2bf(float f) { return (unsigned)__builtin_bit_cast(u16, (__bf16)f); }
__device__ __forceinline__ unsigned pk2bf(float lo, float hi) { bf16x2 v = {(__bf16)lo, (__bf16)hi}; return __builtin_bit_cast(unsigned, v); }
__device__ __forceinline__ u64 ld_gran(gu64* g) { return __hip_atomic_load(g, RLX_AGENT); }
__device__ __forceinline__ __amdgpu_buffer_rsrc_t mk_rsrc(const void* p, unsigned bytes) {
  return __builtin_amdgcn_make_buffer_rsrc(const_cast<void*>(p), 0, bytes, 0x00020000);
}
__device__ __forceinline__ f32x4 mfma16(v4u a, v4u b, f32x4 c) {
  return __builtin_amdgcn_mfma_f32_16x16x32_bf16(__builtin_bit_cast(bf16x8, a), __builtin_bit_cast(bf16x8, b), c, 0, 0, 0);
}
__device__ __forceinline__ v2u tr_read(const unsigned char* p) {
  return __builtin_bit_cast(v2u, __builtin_amdgcn_ds_read_tr16_b64_v4i16((LAS v4i16_t*)p));
}
#define SB() __builtin_amdgcn_sched_barrier(0)
#define WAITFOR(v) do { asm volatile("" ::"v"(v)); SB(); } while (0)

__global__ void __launch_bounds__(NTHR, 1) gated_mix_decode_kernel(Params p) {
  __shared__ __attribute__((aligned(16))) unsigned char smem[L_BYTES];
  const int tid = threadIdx.x, lane = tid & 63, w = __builtin_amdgcn_readfirstlane(tid >> 6), r = lane >> 3, c = lane & 7;
  const int wg = blockIdx.x, bh = (wg & 7) * 16 + (wg >> 4), s = (wg >> 3) & 1, b = bh >> 5, h = bh & 31;
  constexpr int W = WINDOW;
  unsigned tzero = (unsigned)__builtin_amdgcn_s_memrealtime();
  u64 pc0;
  asm volatile("s_getpc_b64 %0" : "=s"(pc0));
  unsigned zoff = 0;
  asm volatile("" : "+v"(zoff));
  const unsigned ep = __hip_atomic_load((gu32*)(p.ws + WS_EPOCH + zoff), RLX_AGENT);
  const bool has1 = wg >= 16;
  const int u1 = has1 ? wg - 16 : 0, lc = u1 >> 1, half = u1 & 1;
  const bool isq = lc < 96;
  const __amdgpu_buffer_rsrc_t rs1 = mk_rsrc(isq ? p.wq : p.w1, has1 ? (isq ? DM * NQ * 2 : DM * DFF * 2) : 0);
  const unsigned ld1 = isq ? NQ * 2 : DFF * 2;
  const unsigned vo1 = (320 * half + 8 * w + r) * ld1 + (isq ? lc : lc - 96) * 128 + 16 * c;
  const size_t kvoff = ((size_t)bh * SK + (SK - WIN) + 512 * s) * DH;
  const __amdgpu_buffer_rsrc_t rsK = mk_rsrc(p.kc + kvoff, 512 * DH * 2), rsV = mk_rsrc(p.vc + kvoff, 512 * DH * 2);
  const unsigned vkv = w * 3072 + r * 384 + c * 16;
  const bool has3 = wg < 200;
  const int rb = wg / 10, cb = wg - rb * 10;
  const bool iso = rb < 16;
  const __amdgpu_buffer_rsrc_t rs3 = mk_rsrc(iso ? p.wo : p.w2, has3 ? (iso ? NQ * DM * 2 : DFF * DM * 2) : 0);
  const unsigned vo3 = ((iso ? rb : rb - 16) * 384 + 8 * w + r) * (DM * 2) + cb * 128 + 16 * c;
  const __amdgpu_buffer_rsrc_t rsW = mk_rsrc(p.ws, (unsigned)WS_END);
  const __amdgpu_buffer_rsrc_t rsX = mk_rsrc(p.x, NB * DM * 2);
  v4u W1[10], K[48], V[48], W3[12], XA[10];
#define ISSUE(n_) do { const int n__ = (n_); \
    if (n__ >= PACE_FROM && n__ < 118 && ((n__ + PACE_PHASE) % PACE_EVERY) == 0) { \
      const unsigned tgt_ = tzero + (unsigned)(PACE_HEAD + ((n__ * PACE) >> 4)); \
      while ((int)((unsigned)__builtin_amdgcn_s_memrealtime() - tgt_) < 0) __builtin_amdgcn_s_sleep(1); \
      SB(); } \
    if (n__ < 10) W1[n__] = __builtin_amdgcn_raw_buffer_load_b128(rs1, vo1, n__ * 32 * ld1, AUX_NT); \
    else if (n__ < 58) K[n__ - 10] = __builtin_amdgcn_raw_buffer_load_b128(rsK, vkv + ((n__ - 10) % 3) * 128, ((n__ - 10) / 3) * 12288, AUX_NT); \
    else if (n__ < 106) V[n__ - 58] = __builtin_amdgcn_raw_buffer_load_b128(rsV, vkv + ((n__ - 58) % 3) * 128, ((n__ - 58) / 3) * 12288, AUX_NT); \
    else if (n__ < 118) W3[n__ - 106] = __builtin_amdgcn_raw_buffer_load_b128(rs3, vo3, (n__ - 106) * 32 * (DM * 2), AUX_NT); \
    SB(); } while (0)
  const unsigned wt_wr = L_WT + (8 * w + r) * 128 + ((((c >> 1) ^ (((r >> 1) & 1) | ((w & 1) << 1))) << 5) | ((c & 1) << 4));
  const int g4 = lane >> 4, q4 = (lane & 15) >> 2, p4 = lane & 3;
  const unsigned wt_rd = L_WT + (8 * g4 + q4) * 128 + (((w ^ (((q4 >> 1) & 1) | ((g4 & 1) << 1))) << 5) | (p4 << 3));

#pragma unroll
  for (int n = 0; n < 10; ++n) ISSUE(n);
  {
    const unsigned xo = (q4 * DM + 320 * half + 8 * g4) * 2;
#pragma unroll
    for (int i = 0; i < 10; ++i) { XA[i] = __builtin_amdgcn_raw_buffer_load_b128(rsX, xo + 64 * i, 0, 0); SB(); }
  }
#pragma unroll
  for (int n = 10; n < W; ++n) ISSUE(n);
#pragma unroll
  for (int i = 0; i < 10; ++i) {
    *reinterpret_cast<v4u*>(smem + wt_wr + i * 4096) = W1[i];
    SB();
    ISSUE(W + i);
  }
  const unsigned tag = ep * 0x9E3779B1u + 0x7F4A7C15u;
  __syncthreads();
  {
    f32x4 d = {0.f, 0.f, 0.f, 0.f};
#pragma unroll
    for (int ks = 0; ks < 10; ++ks) {
      const v2u t0 = tr_read(smem + wt_rd + ks * 4096), t1 = tr_read(smem + wt_rd + ks * 4096 + 512);
      d = mfma16(XA[ks], v4u{t0.x, t0.y, t1.x, t1.y}, d);
    }
    const unsigned off = has1 ? (unsigned)(WS_S1 + (size_t)((g4 * 2 + half) * NCOL + 64 * lc + 16 * w + (lane & 15)) * 8) : OOB;
    __builtin_amdgcn_raw_buffer_store_b64(v2u{__float_as_uint(d[0]), tag}, rsW, off, 0, AUX_SC1);
    __builtin_amdgcn_raw_buffer_store_b64(v2u{tzero, tag}, rsW, (wg == 0 && tid == 0) ? (unsigned)WS_T0 : OOB, 0, AUX_SC1);
    SB();
  }
  u64 t0g = 0;
#pragma unroll
  for (int n = W + 10; n <= 10 + QPOS; ++n) {
    WAITFOR(K[n - W - 10]); ISSUE(n);
    if (n == W + 10 + TB_RD) { t0g = ld_gran((gu64*)(p.ws + WS_T0)); SB(); }
  }
  gu64* qg = (gu64*)(p.ws + WS_S1) + (size_t)b * 2 * NCOL + h * DH + lane;
  u64 qv[6];
#pragma unroll
  for (int j = 0; j < 3; ++j) { qv[2 * j] = ld_gran(qg + 64 * j); qv[2 * j + 1] = ld_gran(qg + NCOL + 64 * j); }
  SB();
#pragma unroll
  for (int n = 10 + QPOS + 1; n <= 10 + QPOS + W; ++n) { WAITFOR(K[n - W - 10]); ISSUE(n); }
  {
    bool ok = true;
#pragma unroll
    for (int j = 0; j < 6; ++j) ok &= (unsigned)(qv[j] >> 32) == tag;
    if (__builtin_expect(!__all(ok), 0)) {
      unsigned spins = 0;
      do {
        __builtin_amdgcn_s_sleep(2);
#pragma unroll
        for (int j = 0; j < 3; ++j) { qv[2 * j] = ld_gran(qg + 64 * j); qv[2 * j + 1] = ld_gran(qg + NCOL + 64 * j); }
        ok = true;
#pragma unroll
        for (int j = 0; j < 6; ++j) ok &= (unsigned)(qv[j] >> 32) == tag;
      } while (!__all(ok) && ++spins < SPIN_CAP);
    }
  }
  {
    const unsigned tv = __builtin_amdgcn_readfirstlane((unsigned)t0g), tg = __builtin_amdgcn_readfirstlane((unsigned)(t0g >> 32));
    const int d = (int)(tzero - tv);
    if (tg == (unsigned)__builtin_amdgcn_readfirstlane(tag) && d > 0 && d < 300) tzero = tv;
  }
  v4u qp[3];
  {
    u16* ql = reinterpret_cast<u16*>(smem + L_Q + w * 384);
#pragma unroll
    for (int j = 0; j < 3; ++j) ql[64 * j + lane] = (u16)f2bf(__uint_as_float((unsigned)qv[2 * j]) + __uint_as_float((unsigned)qv[2 * j + 1]));
    asm volatile("s_waitcnt lgkmcnt(0)" ::: "memory");
#pragma unroll
    for (int j = 0; j < 3; ++j) qp[j] = *reinterpret_cast<const v4u*>(smem + L_Q + w * 384 + j * 128 + c * 16);
  }
  float sc[16];
#define QK_GROUP(i) do { float d_ = 0.f; \
    _Pragma("unroll") for (int j = 0; j < 3; ++j) _Pragma("unroll") for (int k = 0; k < 4; ++k) d_ = dot2(K[3 * (i) + j][k], qp[j][k], d_); \
    sc[i] = d_; asm volatile("" : "+v"(sc[i])); SB(); } while (0)
  constexpr int G0 = (QPOS + 1) / 3;
#pragma unroll
  for (int i = 0; i < G0; ++i) QK_GROUP(i);
#pragma unroll
  for (int m = 3 * G0; m < 48; ++m) {
    if (m % 3 == 2) { QK_GROUP(m / 3); } else if (m > QPOS) { WAITFOR(K[m]); }
    if (m > QPOS) ISSUE(10 + m + W);
  }
#define TR_STEP(BASE, HALF, CTRL, BIT) _Pragma("unroll") for (int q_ = 0; q_ < (HALF); ++q_) { \
    const float t1_ = sc[BASE + q_] + dpp<CTRL>(sc[BASE + q_]), t2_ = sc[BASE + q_ + (HALF)] + dpp<CTRL>(sc[BASE + q_ + (HALF)]); sc[BASE + q_] = (c & (BIT)) ? t2_ : t1_; }
  TR_STEP(0, 4, XOR7, 4) TR_STEP(0, 2, XOR2, 2) TR_STEP(0, 1, XOR1, 1)
  TR_STEP(8, 4, XOR7, 4) TR_STEP(8, 2, XOR2, 2) TR_STEP(8, 1, XOR1, 1)
#undef TR_STEP
  const float sa = sc[0] * SCL2, sb = sc[8] * SCL2;
  const float mw = wave_max(fmaxf(sa, sb));
  const float pa = __builtin_amdgcn_exp2f(sa - mw), pb = __builtin_amdgcn_exp2f(sb - mw);
  const float lw = wave_sum(pa + pb);
  {
    const unsigned off = lane < 2 ? (unsigned)(WS_ML + (size_t)(bh * 16 + (s * 4 + w) * 2 + lane) * 8) : OOB;
    __builtin_amdgcn_raw_buffer_store_b64(v2u{__float_as_uint(lane == 0 ? mw : lw), tag}, rsW, off, 0, AUX_SC1);
  }
  *reinterpret_cast<float*>(smem + L_P + (w * 128 + lane) * 4) = pa;
  *reinterpret_cast<float*>(smem + L_P + (w * 128 + 64 + lane) * 4) = pb;
  asm volatile("s_waitcnt lgkmcnt(0)" ::: "memory");
  f32x4 pv[4];
  pv[0] = *reinterpret_cast<const f32x4*>(smem + L_P + (w * 128 + 8 * r) * 4);
  pv[1] = *reinterpret_cast<const f32x4*>(smem + L_P + (w * 128 + 8 * r + 4) * 4);
  pv[2] = *reinterpret_cast<const f32x4*>(smem + L_P + (w * 128 + 64 + 8 * r) * 4);
  pv[3] = *reinterpret_cast<const f32x4*>(smem + L_P + (w * 128 + 64 + 8 * r + 4) * 4);
  gu64* mlg = (gu64*)(p.ws + WS_ML) + (size_t)bh * 16 + (lane & 15);
  u64 mlv = 0;
  unsigned cpf = 0;
  float fac = 0.f;
  f32x2 acc[3][4];
#pragma unroll
  for (int j = 0; j < 3; ++j)
#pragma unroll
    for (int k = 0; k < 4; ++k) acc[j][k] = f32x2{0.f, 0.f};
#pragma unroll
  for (int m = 0; m < 48; ++m) {
    const int i = m / 3, j = m % 3;
    const float pi = pv[i >> 2][i & 3];
    const f32x2 pp = {pi, pi};
#pragma unroll
    for (int k = 0; k < 4; ++k) acc[j][k] = __builtin_elementwise_fma(pp, f32x2{f_lo(V[m][k]), f_hi(V[m][k])}, acc[j][k]);
    asm volatile("" : "+v"(acc[j][0]), "+v"(acc[j][1]), "+v"(acc[j][2]), "+v"(acc[j][3]));
    SB();
    ISSUE(58 + m + W);
    if (m + W == MLPOS) {
      mlv = ld_gran(mlg);
      cpf = __builtin_amdgcn_raw_buffer_load_b32(mk_rsrc((const void*)(pc0 + CODE_PF_OFF), w == 0 ? CODE_PF_LEN : 0), lane * 128, 0, 0);
      SB();
    }
    if (m == MLPOS + 1) {
      if (__builtin_expect(!__all((unsigned)(mlv >> 32) == tag), 0)) {
        unsigned spins = 0;
        do {
          __builtin_amdgcn_s_sleep(1);
          mlv = ld_gran(mlg);
        } while (!__all((unsigned)(mlv >> 32) == tag) && ++spins < SPIN_CAP);
      }
      const float val = __uint_as_float((unsigned)mlv);
      const bool odd = lane & 1;
      const float M = wave_max(odd ? -INFINITY : val);
      const float mp = dpp<XOR1>(val);
      const float t = (odd && lane < 16) ? __builtin_amdgcn_exp2f(mp - M) * val : 0.f;
      const float L = wave_sum(t);
      fac = __builtin_amdgcn_exp2f(mw - M) / L;
      asm volatile("" : "+v"(fac));
      asm volatile("" ::"v"(cpf));
      SB();
    }
    if (m >= 45) {
      float a4[4], a2[2];
#pragma unroll
      for (int e = 0; e < 4; ++e) a4[e] = swap32_add(acc[j][e >> 1][e & 1], acc[j][(e + 4) >> 1][e & 1]);
#pragma unroll
      for (int e = 0; e < 2; ++e) a2[e] = swap16_add(a4[e], a4[e + 2]);
      const float t1 = a2[0] + dpp<XOR8>(a2[0]), t2 = a2[1] + dpp<XOR8>(a2[1]);
      *reinterpret_cast<float*>(smem + L_O + (w * 192 + 64 * j + 8 * c + r) * 4) = ((lane & 8) ? t2 : t1) * fac;
      SB();
    }
  }
#pragma unroll
  for (int n = 58 + 48 + W; n < 118; ++n) ISSUE(n);
  __syncthreads();
  {
    const int t = tid < 96 ? tid : 95;
    f32x2 a = {0.f, 0.f};
#pragma unroll
    for (int ww = 0; ww < 4; ++ww) a += *reinterpret_cast<const f32x2*>(smem + L_O + (ww * 192 + 2 * t) * 4);
    const unsigned off = tid < 96 ? (unsigned)(WS_OG + (size_t)((b * 2 + s) * (NQ / 2) + h * (DH / 2) + tid) * 8) : OOB;
    __builtin_amdgcn_raw_buffer_store_b64(v2u{pk2bf(a.x, a.y), tag}, rsW, off, 0, AUX_SC1);
    SB();
  }
  if (__builtin_expect(has3, 1)) {
    v4u bf[12], af[12];
    const unsigned pgo = (unsigned)(WS_PG + (size_t)((rb * 10 + cb) * 256 + g4 * 64 + 16 * w + (lane & 15)) * 8);
    if (__builtin_expect(iso, 1)) {
      const int i1 = tid < 128 ? tid + 256 : tid, b0 = tid / 96, k0 = tid - b0 * 96, b1 = i1 / 96, k1 = i1 - b1 * 96;
      const unsigned zo0 = (unsigned)(WS_OG + (size_t)(b0 * 2 * (NQ / 2) + rb * 192 + 2 * k0) * 8);
      const unsigned zo1 = (unsigned)(WS_OG + (size_t)(b1 * 2 * (NQ / 2) + rb * 192 + 2 * k1) * 8);
      const unsigned zl0 = L_Z + b0 * 832 + k0 * 8, zl1 = L_Z + b1 * 832 + k1 * 8;
      v4u A[4], B[4];
#define ZISSUE(S) do { asm volatile("" ::: "memory");   \
        S[0] = __builtin_amdgcn_raw_buffer_load_b128(rsW, zo0, 0, AUX_SC1); S[1] = __builtin_amdgcn_raw_buffer_load_b128(rsW, zo0, (NQ / 2) * 8, AUX_SC1); \
        S[2] = __builtin_amdgcn_raw_buffer_load_b128(rsW, zo1, 0, AUX_SC1); S[3] = __builtin_amdgcn_raw_buffer_load_b128(rsW, zo1, (NQ / 2) * 8, AUX_SC1); SB(); } while (0)
#define ZOK(S) __all((((S[0].y ^ tag) | (S[0].w ^ tag) | (S[1].y ^ tag)) | ((S[1].w ^ tag) | (S[2].y ^ tag) | (S[2].w ^ tag)) | ((S[3].y ^ tag) | (S[3].w ^ tag))) == 0u)
#define ZPUT(S) do { \
        v2u z0 = {pk2bf(f_lo(S[0].x) + f_lo(S[1].x), f_hi(S[0].x) + f_hi(S[1].x)), pk2bf(f_lo(S[0].z) + f_lo(S[1].z), f_hi(S[0].z) + f_hi(S[1].z))}; \
        *reinterpret_cast<v2u*>(smem + zl0) = z0; \
        v2u z1 = {pk2bf(f_lo(S[2].x) + f_lo(S[3].x), f_hi(S[2].x) + f_hi(S[3].x)), pk2bf(f_lo(S[2].z) + f_lo(S[3].z), f_hi(S[2].z) + f_hi(S[3].z))}; \
        *reinterpret_cast<v2u*>(smem + zl1) = z1; SB(); } while (0)
#pragma unroll
      for (int i = 0; i < ZPOS; ++i) { *reinterpret_cast<v4u*>(smem + wt_wr + i * 4096) = W3[i]; SB(); }
      ZISSUE(A);
#pragma unroll
      for (int i = ZPOS; i < 12; ++i) { *reinterpret_cast<v4u*>(smem + wt_wr + i * 4096) = W3[i]; SB(); }
      ZISSUE(B);
      __syncthreads();
#pragma unroll
      for (int ks = 0; ks < 12; ++ks) {
        const v2u t0 = tr_read(smem + wt_rd + ks * 4096), t1 = tr_read(smem + wt_rd + ks * 4096 + 512);
        bf[ks] = v4u{t0.x, t0.y, t1.x, t1.y};
      }
      SB();
      if (__builtin_expect(ZOK(A), 1)) { ZPUT(A); }
      else if (__builtin_expect(ZOK(B), 1)) { ZPUT(B); }
      else {
        v4u C[4];
        unsigned spins = 0;
        do { ZISSUE(C); } while (!ZOK(C) && ++spins < SPIN_CAP);
        ZPUT(C);
      }
      __syncthreads();
#pragma unroll
      for (int ks = 0; ks < 12; ++ks) af[ks] = *reinterpret_cast<const v4u*>(smem + L_Z + q4 * 832 + (32 * ks + 8 * g4) * 2);
      SB();
      f32x4 d = {0.f, 0.f, 0.f, 0.f}, d2 = {0.f, 0.f, 0.f, 0.f};
#pragma unroll
      for (int ks = 0; ks < 12; ks += 2) { d = mfma16(af[ks], bf[ks], d); d2 = mfma16(af[ks + 1], bf[ks + 1], d2); }
      __builtin_amdgcn_raw_buffer_store_b64(v2u{__float_as_uint(d[0] + d2[0]), tag}, rsW, pgo, 0, AUX_SC1);
      SB();
#pragma unroll
      for (int k = 0; k < 4; ++k) { asm volatile("" ::"v"(A[k])); asm volatile("" ::"v"(B[k])); }
    } else {
      gu64* src = (gu64*)(p.ws + WS_S1) + NQ + (rb - 16) * 384;
      u64 zv[12];
      int zo[6];
#pragma unroll
      for (int k = 0; k < 6; ++k) { const int pi = tid + 256 * k, bb = pi / 384, row = pi - bb * 384; zo[k] = bb * 2 * NCOL + row; }
#pragma unroll
      for (int k = 0; k < 6; ++k) { zv[2 * k] = ld_gran(src + zo[k]); zv[2 * k + 1] = ld_gran(src + zo[k] + NCOL); }
      SB();
#pragma unroll
      for (int i = 0; i < 12; ++i) *reinterpret_cast<v4u*>(smem + wt_wr + i * 4096) = W3[i];
      __syncthreads();
#pragma unroll
      for (int ks = 0; ks < 12; ++ks) {
        const v2u t0 = tr_read(smem + wt_rd + ks * 4096), t1 = tr_read(smem + wt_rd + ks * 4096 + 512);
        bf[ks] = v4u{t0.x, t0.y, t1.x, t1.y};
      }
      SB();
      bool ok = true;
#pragma unroll
      for (int k = 0; k < 12; ++k) ok &= (unsigned)(zv[k] >> 32) == tag;
      if (__builtin_expect(!__all(ok), 0)) {
        unsigned spins = 0;
        do {
#pragma unroll
          for (int k = 0; k < 6; ++k) { zv[2 * k] = ld_gran(src + zo[k]); zv[2 * k + 1] = ld_gran(src + zo[k] + NCOL); }
          ok = true;
#pragma unroll
          for (int k = 0; k < 12; ++k) ok &= (unsigned)(zv[k] >> 32) == tag;
        } while (!__all(ok) && ++spins < SPIN_CAP);
      }
#pragma unroll
      for (int k = 0; k < 6; ++k) {
        const int pi = tid + 256 * k, bb = pi / 384, row = pi - bb * 384;
        const float z = fmaxf(__uint_as_float((unsigned)zv[2 * k]) + __uint_as_float((unsigned)zv[2 * k + 1]), 0.f);
        *reinterpret_cast<u16*>(smem + L_Z + bb * 832 + row * 2) = (u16)f2bf(z);
      }
      __syncthreads();
#pragma unroll
      for (int ks = 0; ks < 12; ++ks) af[ks] = *reinterpret_cast<const v4u*>(smem + L_Z + q4 * 832 + (32 * ks + 8 * g4) * 2);
      SB();
      f32x4 d = {0.f, 0.f, 0.f, 0.f}, d2 = {0.f, 0.f, 0.f, 0.f};
#pragma unroll
      for (int ks = 0; ks < 12; ks += 2) { d = mfma16(af[ks], bf[ks], d); d2 = mfma16(af[ks + 1], bf[ks + 1], d2); }
      __builtin_amdgcn_raw_buffer_store_b64(v2u{__float_as_uint(d[0] + d2[0]), tag}, rsW, pgo, 0, AUX_SC1);
    }
  } else if (wg < 240) {
    const int item = (wg - 200) * 4 + w, fcb = item >> 4, fb = (item >> 2) & 3, fq = item & 3;
    float gs = 0.f;
#pragma unroll
    for (int k = 0; k < 10; ++k) gs = fmaf(bf2f(p.x[fb * DM + 64 * k + lane]), bf2f(p.gw[64 * k + lane]), gs);
    gs = wave_sum(gs);
    float g = 1.f / (1.f + __expf(-gs));
    asm volatile("" : "+v"(g));
    const unsigned po = (unsigned)(WS_PG + (size_t)((g4 * 10 + fcb) * 256 + fb * 64 + fq * 16 + (lane & 15)) * 8);
    v2u S[5];
    {
      unsigned spins = 0;
      bool ok;
      do {
        asm volatile("" ::: "memory");
#pragma unroll
        for (int i = 0; i < 5; ++i) S[i] = __builtin_amdgcn_raw_buffer_load_b64(rsW, po, i * (4 * 10 * 256 * 8), AUX_SC1);
        ok = __all((((S[0].y ^ tag) | (S[1].y ^ tag) | (S[2].y ^ tag)) | ((S[3].y ^ tag) | (S[4].y ^ tag))) == 0u);
      } while (!ok && ++spins < SPIN_CAP);
    }
    float res = g * ((__uint_as_float(S[0].x) + __uint_as_float(S[1].x)) + (__uint_as_float(S[2].x) + __uint_as_float(S[3].x))) + (1.f - g) * __uint_as_float(S[4].x);
    {
      auto t16 = __builtin_amdgcn_permlane16_swap(__float_as_uint(res), __float_as_uint(res), false, false);
      res = __uint_as_float(t16[0]) + __uint_as_float(t16[1]);
      auto t32 = __builtin_amdgcn_permlane32_swap(__float_as_uint(res), __float_as_uint(res), false, false);
      res = __uint_as_float(t32[0]) + __uint_as_float(t32[1]);
    }
    if (lane < 16) p.out[fb * DM + fcb * 64 + fq * 16 + lane] = (u16)f2bf(res);
    if (item == 0 && lane == 0) __hip_atomic_store((gu32*)(p.ws + WS_EPOCH), ep + 1u, RLX_AGENT);
  }
}

extern "C" void kernel_launch(void* const* d_in, const int* in_sizes, int n_in, void* d_out, int out_size, void* d_ws, size_t ws_size, hipStream_t stream) {
  Params p;
  p.x = (const u16*)d_in[0]; p.kc = (const u16*)d_in[1]; p.vc = (const u16*)d_in[2]; p.wq = (const u16*)d_in[3]; p.wo = (const u16*)d_in[4];
  p.w1 = (const u16*)d_in[6]; p.w2 = (const u16*)d_in[7]; p.gw = (const u16*)d_in[8]; p.out = (u16*)d_out; p.ws = (unsigned char*)d_ws;
  hipLaunchKernelGGL(gated_mix_decode_kernel, dim3(NWG), dim3(NTHR), 0, stream, p);
}
```
